```python
import torch

def _softmax(x, axis=-1):
    return torch.softmax(x, dim=axis)
def _gelu(x):
    return torch.nn.functional.gelu(x, approximate="tanh")
def _silu(x):
    return torch.nn.functional.silu(x)
def _sigmoid(x):
    return torch.sigmoid(x)
def _layernorm(x, axis=-1, eps=1e-5):
    m = x.mean(dim=axis, keepdim=True)
    v = x.var(dim=axis, keepdim=True, unbiased=False)
    return (x - m) / torch.sqrt(v + eps)
def _scatter_add(tgt, idx, src, axis=0):
    return tgt.clone().index_add_(axis, idx, src)
def _segment_sum(src, seg, n):
    out = torch.zeros((n,) + src.shape[1:], dtype=src.dtype)
    return out.index_add_(0, seg, src)
def _pad(x, pw):
    flat = tuple(v for pair in reversed(pw) for v in pair)
    return torch.nn.functional.pad(x, flat)

kernel_name = 'parallel_sum__sliding_block_cached__mixer_token_mlp__decode'

def setup_inputs():

    _w = (lambda a: (lambda t: t.to(torch.bfloat16) if t.is_floating_point() else t)(torch.as_tensor(a)))
    def _A_setup():
        B, Sq, D = 1, 1, 448
        Hq, Hkv, Sk, Dh = 16, 8, 32768, 256
        assert Hq % Hkv == 0, (Hq, Hkv)
        return {
            "x": _w((lambda *s: torch.randn(*s, dtype=torch.bfloat16) * 0.05)(B, Sq, D)),
            "k_cache": _w((lambda *s: torch.randn(*s, dtype=torch.bfloat16) * 1.6)(B, Hkv, Sk, Dh)),
            "v_cache": _w((lambda *s: torch.randn(*s, dtype=torch.bfloat16) * 0.8)(B, Hkv, Sk, Dh)),
            "wq": _w((lambda *s: torch.randn(*s, dtype=torch.bfloat16) * 1.6)(D, Hq * Dh) * (D ** -0.5)),
            "wo": _w((lambda *s: torch.randn(*s, dtype=torch.bfloat16) * 0.8)(Hq * Dh, D) * ((Hq * Dh) ** -0.5)),
        }
    _A_in = _A_setup()

    def _B_setup():
        B, S, D, F = 1, 1, 448, 1792
        return {
            "x": _w((lambda *s: torch.randn(*s, dtype=torch.bfloat16) * 0.05)(B, S, D)),
            "w1": _w((lambda *s: torch.randn(*s, dtype=torch.bfloat16) * 0.4)(S, F) * (S ** -0.5)),
            "b1": _w((lambda *s: torch.randn(*s, dtype=torch.bfloat16) * 0.4)(F) * 0.02),
            "w2": _w((lambda *s: torch.randn(*s, dtype=torch.bfloat16) * 0.4)(F, S) * (F ** -0.5)),
            "b2": _w((lambda *s: torch.randn(*s, dtype=torch.bfloat16) * 0.4)(S) * 0.02),
        }
    _B_in = _B_setup()

    return {
        "A_x": _A_in["x"],
        "A_k_cache": _A_in["k_cache"],
        "A_v_cache": _A_in["v_cache"],
        "A_wq": _A_in["wq"],
        "A_wo": _A_in["wo"],
        "B_x": _B_in["x"],
        "B_w1": _B_in["w1"],
        "B_b1": _B_in["b1"],
        "B_w2": _B_in["w2"],
        "B_b2": _B_in["b2"],
    }

def reference(A_x, A_k_cache, A_v_cache, A_wq, A_wo, B_x, B_w1, B_b1, B_w2, B_b2):
    def _A_ref(x, k_cache, v_cache, wq, wo):
        B, Sq, D = x.shape
        Hq, Dh = 16, 256
        Hkv, Sk = k_cache.shape[1], k_cache.shape[2]
        W = min(4096, max(256, Sk // 4))
        rep = Hq // Hkv
        q = (lambda x, *d: x.permute(*d))((x @ wq).reshape(B, Sq, Hq, Dh), 0, 2, 1, 3)
        k = (lambda x, n, axis: torch.repeat_interleave(x, n, dim=axis))(k_cache[:, :, Sk - W:, :], rep, axis=1)
        v = (lambda x, n, axis: torch.repeat_interleave(x, n, dim=axis))(v_cache[:, :, Sk - W:, :], rep, axis=1)
        scores = torch.einsum("bhqd,bhkd->bhqk", q, k) * (Dh ** -0.5)
        p = _softmax(scores, axis=-1)
        o = torch.einsum("bhqk,bhkd->bhqd", p, v)
        return (lambda x, *d: x.permute(*d))(o, 0, 2, 1, 3).reshape(B, Sq, Hq * Dh) @ wo

    def _B_ref(x, w1, b1, w2, b2):
        B, S, D = x.shape
        h = _layernorm(x, axis=-1)
        u = torch.einsum("bsd,sf->bdf", h, w1) + b1
        u = _gelu(u)
        y = torch.einsum("bdf,fs->bds", u, w2) + b2
        return (lambda x, *d: x.permute(*d))(y, 0, 2, 1)

    x = A_x
    xn = _layernorm(x, axis=-1)
    return x + _A_ref(xn, A_k_cache, A_v_cache, A_wq, A_wo) + _B_ref(xn, B_w1, B_b1, B_w2, B_b2)
```

```cpp
#include <hip/hip_runtime.h>
#include <hip/hip_bf16.h>
#include <stdint.h>

typedef unsigned u32x4 __attribute__((ext_vector_type(4)));
typedef unsigned u32x2 __attribute__((ext_vector_type(2)));
typedef float f32x4 __attribute__((ext_vector_type(4)));
typedef __bf16 bf16x2 __attribute__((ext_vector_type(2)));
#define GAS __attribute__((address_space(1)))
typedef GAS unsigned long long gu64;
typedef GAS unsigned gu32;
#define RLX_AGENT __ATOMIC_RELAXED, __HIP_MEMORY_SCOPE_AGENT

namespace {
constexpr int DM = 448, SK = 32768, DH = 256, WIN = 4096, FF = 1792;
constexpr int NWG = 256, NTHR = 576;
constexpr unsigned WS_EPOCH = 0;
constexpr unsigned WS_Q1 = 4096;
constexpr unsigned WS_ML = WS_Q1 + 8 * 4 * 512 * 8;
constexpr unsigned WS_O2 = WS_ML + 8 * 32 * 4 * 8;
constexpr unsigned WS_P3 = WS_O2 + 8 * 32 * 512 * 8;
constexpr unsigned WS_END = WS_P3 + 4 * 56 * 64 * 16;
constexpr unsigned SPIN_CAP = 1u << 20;
constexpr float LOG2E = 1.4426950408889634f;

constexpr int L_XN = 0;
constexpr int L_QP = L_XN + 9 * 448;
constexpr int L_QS = L_QP + 7 * 64;
constexpr int L_S = L_QS + 256;
constexpr int L_MLP = L_S + 256;
constexpr int L_R = L_MLP + 16;
constexpr int L_T = L_R + 8 * 64;
constexpr int L_OM = L_T + 8 * 128;
constexpr int L_END = L_OM + 8 * 64 * 16;

__device__ __forceinline__ float bf_lo(unsigned w) { return __builtin_bit_cast(float, w << 16); }
__device__ __forceinline__ float bf_hi(unsigned w) { return __builtin_bit_cast(float, w & 0xffff0000u); }
__device__ __forceinline__ float rbf(float f) {
  return (float)(__bf16)f;
}
__device__ __forceinline__ unsigned pack_bf(float lo, float hi) {
  bf16x2 v; v.x = (__bf16)lo; v.y = (__bf16)hi; return __builtin_bit_cast(unsigned, v);
}
__device__ __forceinline__ float dot2(unsigned a, unsigned b, float acc) {
  return __builtin_amdgcn_fdot2_f32_bf16(__builtin_bit_cast(bf16x2, a), __builtin_bit_cast(bf16x2, b), acc, false);
}
template <int CTRL> __device__ __forceinline__ float dpp(float x) {
  return __builtin_bit_cast(float, __builtin_amdgcn_mov_dpp(__builtin_bit_cast(int, x), CTRL, 0xf, 0xf, true));
}
constexpr int XOR1 = 0xB1, XOR2 = 0x4E, XOR7 = 0x141, XOR8 = 0x128;
__device__ __forceinline__ float tr16(float lo, float hi) {
  auto s = __builtin_amdgcn_permlane16_swap(__float_as_uint(lo), __float_as_uint(hi), false, false);
  return __uint_as_float(s[0]) + __uint_as_float(s[1]);
}
__device__ __forceinline__ float tr32(float lo, float hi) {
  auto s = __builtin_amdgcn_permlane32_swap(__float_as_uint(lo), __float_as_uint(hi), false, false);
  return __uint_as_float(s[0]) + __uint_as_float(s[1]);
}
template <int CTRL> __device__ __forceinline__ float trd(float lo, float hi, bool bit) {
  const float keep = bit ? hi : lo, send = bit ? lo : hi;
  return keep + dpp<CTRL>(send);
}
__device__ __forceinline__ float sum32(float v) {
  v += dpp<XOR1>(v); v += dpp<XOR2>(v); v += dpp<XOR7>(v); v += dpp<XOR8>(v);
  return tr16(v, v);
}
__device__ __forceinline__ float sum64(float v) { v = sum32(v); return tr32(v, v); }
__device__ __forceinline__ float max32(float v) {
  v = fmaxf(v, dpp<XOR1>(v)); v = fmaxf(v, dpp<XOR2>(v)); v = fmaxf(v, dpp<XOR7>(v)); v = fmaxf(v, dpp<XOR8>(v));
  auto s = __builtin_amdgcn_permlane16_swap(__float_as_uint(v), __float_as_uint(v), false, false);
  return fmaxf(__uint_as_float(s[0]), __uint_as_float(s[1]));
}
__device__ __forceinline__ float swap32(float v) {
  auto s = __builtin_amdgcn_permlane32_swap(__float_as_uint(v), __float_as_uint(v), false, false);
  const bool hi = (threadIdx.x & 32) != 0;
  return hi ? __uint_as_float(s[0]) : __uint_as_float(s[1]);
}
__device__ __forceinline__ u32x4 ntload(const void* p) { return __builtin_nontemporal_load((const u32x4*)p); }
#define LGKM0() asm volatile("s_waitcnt lgkmcnt(0)" ::: "memory")
#define BAR() do { asm volatile("s_waitcnt lgkmcnt(0)" ::: "memory"); __builtin_amdgcn_s_barrier(); asm volatile("" ::: "memory"); } while (0)

struct Params {
  const unsigned short* x; const unsigned short* k; const unsigned short* v; const unsigned short* wq; const unsigned short* wo;
  const unsigned short* w1; const unsigned short* b1; const unsigned short* w2; const unsigned short* b2;
  unsigned short* out; unsigned char* ws;
};

__global__ void __launch_bounds__(NTHR, 3) fused_decode(Params p) {
  __shared__ __attribute__((aligned(16))) float lds[L_END];
  const int b = blockIdx.x, g = b & 7, j = b >> 3;
  const int tid = threadIdx.x, lane = tid & 63, w = __builtin_amdgcn_readfirstlane(tid >> 6);
  const int cb = j & 7, ks = j >> 3;
  const int cq = j & 3, rblk = j >> 2;
  const __amdgpu_buffer_rsrc_t wsr = __builtin_amdgcn_make_buffer_rsrc(p.ws, 0, WS_END, 0x00020000);
  gu64* const wsg = (gu64*)p.ws;

  unsigned eoff = WS_EPOCH; asm volatile("" : "+v"(eoff));
  const unsigned tag = __builtin_amdgcn_raw_buffer_load_b32(wsr, eoff, 0, 16) + 1u;
  const u32x4 xr = *(const u32x4*)(p.x + (lane < 56 ? lane : 55) * 8);

  if (w == 8) {
    BAR();
    float qa[8];
    for (unsigned spins = 0;; ++spins) {
      asm volatile("" ::: "memory");
      bool ok = true;
#pragma unroll
      for (int i = 0; i < 4; ++i) {
        float a0 = 0.f, a1 = 0.f;
#pragma unroll
        for (int s = 0; s < 4; ++s) {
          const u32x4 r = __builtin_amdgcn_raw_buffer_load_b128(wsr, WS_Q1 + (unsigned)(((g * 4 + s) * 512 + 2 * (lane + 64 * i)) * 8), 0, 16);
          ok &= (r.y == tag) & (r.w == tag);
          a0 += __uint_as_float(r.x); a1 += __uint_as_float(r.z);
        }
        qa[2 * i] = a0; qa[2 * i + 1] = a1;
      }
      if (__all(ok) || spins > SPIN_CAP) break;
      __builtin_amdgcn_s_sleep(1);
    }
    unsigned* qs = (unsigned*)(lds + L_QS);
#pragma unroll
    for (int i = 0; i < 4; ++i) qs[lane + 64 * i] = pack_bf(qa[2 * i], qa[2 * i + 1]);
    BAR();
    return;
  }

  u32x4 wqr[2], m1r, mbr, m2r, kr[8], vr[8], wor[2];
  {
    const int wr = (w < 7 ? w : 6);
    const unsigned short* s = p.wq + (size_t)(ks * 112 + wr * 16 + (lane >> 3)) * 4096 + g * 512 + cb * 64 + (lane & 7) * 8;
    wqr[0] = ntload(s); wqr[1] = ntload(s + 8 * 4096);
  }
  {
    const int o = 224 * w + (lane < 28 ? lane : 27) * 8;
    m1r = *(const u32x4*)(p.w1 + o); mbr = *(const u32x4*)(p.b1 + o); m2r = *(const u32x4*)(p.w2 + o);
  }
  {
    const size_t o = ((size_t)g * SK + (SK - WIN) + j * 128 + w * 16) * DH + lane * 8;
#pragma unroll
    for (int i = 0; i < 8; ++i) kr[i] = ntload(p.k + o + i * 512);
#pragma unroll
    for (int i = 0; i < 8; ++i) vr[i] = ntload(p.v + o + i * 512);
  }
  {
    const int c = lane & 15;
    const unsigned short* s = p.wo + (size_t)(g * 512 + rblk * 64 + 8 * w + (lane >> 4)) * DM + cq * 112 + (c < 14 ? c : 13) * 8;
    wor[0] = ntload(s); wor[1] = ntload(s + 4 * DM);
  }
  __builtin_amdgcn_sched_barrier(0);

  float xf[8], xn[8];
#pragma unroll
  for (int i = 0; i < 4; ++i) { xf[2 * i] = bf_lo(xr[i]); xf[2 * i + 1] = bf_hi(xr[i]); }
  const float lv = lane < 56 ? 1.f : 0.f;
  {
    float s = 0.f;
#pragma unroll
    for (int i = 0; i < 8; ++i) s += xf[i];
    const float mean = sum64(s * lv) * (1.f / DM);
    float q2 = 0.f;
#pragma unroll
    for (int i = 0; i < 8; ++i) { const float d = xf[i] - mean; q2 += d * d; }
    const float var = sum64(q2 * lv) * (1.f / DM);
    const float mb = rbf(mean), den = rbf(sqrtf(rbf(rbf(var) + 1e-5f)));
#pragma unroll
    for (int i = 0; i < 8; ++i) xn[i] = rbf(rbf(xf[i] - mb) / den);
  }
  float* xl = lds + L_XN + w * 448;
  if (lane < 56) {
    *(f32x4*)(xl + lane * 8) = f32x4{xn[0], xn[1], xn[2], xn[3]};
    *(f32x4*)(xl + lane * 8 + 4) = f32x4{xn[4], xn[5], xn[6], xn[7]};
  }
  LGKM0();
  if (w < 7) {
    float a[8];
    const float x0 = xl[ks * 112 + w * 16 + (lane >> 3)], x1 = xl[ks * 112 + w * 16 + 8 + (lane >> 3)];
#pragma unroll
    for (int i = 0; i < 4; ++i) {
      a[2 * i] = bf_lo(wqr[0][i]) * x0 + bf_lo(wqr[1][i]) * x1;
      a[2 * i + 1] = bf_hi(wqr[0][i]) * x0 + bf_hi(wqr[1][i]) * x1;
    }
    float c4[4], c2[2];
#pragma unroll
    for (int i = 0; i < 4; ++i) c4[i] = tr32(a[i], a[i + 4]);
#pragma unroll
    for (int i = 0; i < 2; ++i) c2[i] = tr16(c4[i], c4[i + 2]);
    const float c1 = trd<XOR8>(c2[0], c2[1], (lane & 8) != 0);
    const int col = (lane & 7) * 8 + ((lane >> 5) & 1) * 4 + ((lane >> 4) & 1) * 2 + ((lane >> 3) & 1);
    lds[L_QP + w * 64 + col] = c1;
  }
  BAR();
  if (w == 0) {
    float s = 0.f;
#pragma unroll
    for (int i = 0; i < 7; ++i) s += lds[L_QP + i * 64 + lane];
    __hip_atomic_store(wsg + (WS_Q1 / 8) + (g * 4 + ks) * 512 + cb * 64 + lane,
                       ((unsigned long long)tag << 32) | __float_as_uint(s), RLX_AGENT);
  }
  const int d0 = (b / 56) * 112 + (b % 56) * 2;
  {
    float s = 0.f;
#pragma unroll
    for (int i = 0; i < 8; ++i) s += xn[i];
    const float mean = sum64(s * lv) * (1.f / DM);
    float q2 = 0.f;
#pragma unroll
    for (int i = 0; i < 8; ++i) { const float d = xn[i] - mean; q2 += d * d; }
    const float var = sum64(q2 * lv) * (1.f / DM);
    const float mb = rbf(mean), den = rbf(sqrtf(rbf(rbf(var) + 1e-5f)));
    const int dd = d0 < DM ? d0 : 0;
    const float h0 = rbf(rbf(xl[dd] - mb) / den), h1 = rbf(rbf(xl[dd + 1] - mb) / den);
    float y0 = 0.f, y1 = 0.f;
#pragma unroll
    for (int i = 0; i < 8; ++i) {
      const float a1 = (i & 1) ? bf_hi(m1r[i >> 1]) : bf_lo(m1r[i >> 1]);
      const float ab = (i & 1) ? bf_hi(mbr[i >> 1]) : bf_lo(mbr[i >> 1]);
      const float a2 = (i & 1) ? bf_hi(m2r[i >> 1]) : bf_lo(m2r[i >> 1]);
      const float u0 = rbf(rbf(h0 * a1) + ab), u1 = rbf(rbf(h1 * a1) + ab);
      const float z0 = 0.7978845608028654f * (u0 + 0.044715f * u0 * u0 * u0);
      const float z1 = 0.7978845608028654f * (u1 + 0.044715f * u1 * u1 * u1);
      const float g0 = rbf(u0 / (1.f + __expf(-2.f * z0))), g1 = rbf(u1 / (1.f + __expf(-2.f * z1)));
      y0 += g0 * a2; y1 += g1 * a2;
    }
    const float mv = lane < 28 ? 1.f : 0.f;
    y0 = sum64(y0 * mv); y1 = sum64(y1 * mv);
    if (lane == 0) { lds[L_MLP + 2 * w] = y0; lds[L_MLP + 2 * w + 1] = y1; }
  }
  BAR();
  {
    const unsigned* qs = (const unsigned*)(lds + L_QS);
    const u32x4 q0 = *(const u32x4*)(qs + (lane & 31) * 4), q1 = *(const u32x4*)(qs + 128 + (lane & 31) * 4);
    float sv[16];
#pragma unroll
    for (int i = 0; i < 8; ++i) {
      float s0 = 0.f, s1 = 0.f;
#pragma unroll
      for (int c = 0; c < 4; ++c) { s0 = dot2(kr[i][c], q0[c], s0); s1 = dot2(kr[i][c], q1[c], s1); }
      sv[2 * i] = s0; sv[2 * i + 1] = s1;
    }
    float t8[8], t4[4], t2[2];
#pragma unroll
    for (int i = 0; i < 8; ++i) t8[i] = tr16(sv[i], sv[i + 8]);
#pragma unroll
    for (int i = 0; i < 4; ++i) t4[i] = trd<XOR8>(t8[i], t8[i + 4], (lane & 8) != 0);
#pragma unroll
    for (int i = 0; i < 2; ++i) t2[i] = trd<XOR7>(t4[i], t4[i + 2], (lane & 4) != 0);
    float t1 = trd<XOR2>(t2[0], t2[1], (lane & 2) != 0);
    t1 += dpp<XOR1>(t1);
    const int ii = ((lane >> 4) & 1) * 4 + ((lane >> 3) & 1) * 2 + ((lane >> 2) & 1), hh = (lane >> 1) & 1;
    if ((lane & 1) == 0) lds[L_S + hh * 128 + w * 16 + 2 * ii + (lane >> 5)] = rbf(t1) * (0.0625f * LOG2E);
  }
  BAR();
  float M0, M1;
  {
    const f32x4 s4 = *(const f32x4*)(lds + L_S + lane * 4);
    const float mh = max32(fmaxf(fmaxf(s4.x, s4.y), fmaxf(s4.z, s4.w)));
    const float mo = swap32(mh);
    M0 = lane < 32 ? mh : mo; M1 = lane < 32 ? mo : mh;
    if (w == 0) {
      float e = __builtin_amdgcn_exp2f(s4.x - mh) + __builtin_amdgcn_exp2f(s4.y - mh) + __builtin_amdgcn_exp2f(s4.z - mh) + __builtin_amdgcn_exp2f(s4.w - mh);
      e = sum32(e);
      if ((lane & 31) == 0) {
        const u32x4 gv = {__float_as_uint(mh), tag, __float_as_uint(e), tag};
        __builtin_amdgcn_raw_buffer_store_b128(gv, wsr, WS_ML + (unsigned)(((g * 32 + j) * 2 + (lane >> 5)) * 16), 0, 16);
      }
    }
  }
  {
    float acc[16];
#pragma unroll
    for (int i = 0; i < 16; ++i) acc[i] = 0.f;
#pragma unroll
    for (int i = 0; i < 8; ++i) {
      const int key = w * 16 + 2 * i + (lane >> 5);
      const float p0 = __builtin_amdgcn_exp2f(lds[L_S + key] - M0), p1 = __builtin_amdgcn_exp2f(lds[L_S + 128 + key] - M1);
#pragma unroll
      for (int c = 0; c < 4; ++c) {
        const float v0 = bf_lo(vr[i][c]), v1 = bf_hi(vr[i][c]);
        acc[2 * c] = fmaf(p0, v0, acc[2 * c]); acc[2 * c + 1] = fmaf(p0, v1, acc[2 * c + 1]);
        acc[8 + 2 * c] = fmaf(p1, v0, acc[8 + 2 * c]); acc[8 + 2 * c + 1] = fmaf(p1, v1, acc[8 + 2 * c + 1]);
      }
    }
    float* om = lds + L_OM + (w * 2 + (lane >> 5)) * 512 + (lane & 31) * 8;
    *(f32x4*)(om) = f32x4{acc[0], acc[1], acc[2], acc[3]};
    *(f32x4*)(om + 4) = f32x4{acc[4], acc[5], acc[6], acc[7]};
    *(f32x4*)(om + 256) = f32x4{acc[8], acc[9], acc[10], acc[11]};
    *(f32x4*)(om + 260) = f32x4{acc[12], acc[13], acc[14], acc[15]};
  }
  BAR();
  {
    float s = 0.f;
#pragma unroll
    for (int i = 0; i < 16; ++i) s += lds[L_OM + i * 512 + tid];
    __hip_atomic_store(wsg + (WS_O2 / 8) + (g * 32 + j) * 512 + tid, ((unsigned long long)tag << 32) | __float_as_uint(s), RLX_AGENT);
  }
  {
    const int hd = rblk >> 2;
    const int pi = 4 * w + (lane >> 4);
    const unsigned oo = WS_O2 + (unsigned)(((g * 32 + pi) * 512 + rblk * 64 + (lane & 15) * 4) * 8);
    const unsigned mo = WS_ML + (unsigned)(((g * 32 + (lane & 31)) * 2 + hd) * 16);
    u32x4 r0, r1, rm;
    for (unsigned spins = 0;; ++spins) {
      asm volatile("" ::: "memory");
      r0 = __builtin_amdgcn_raw_buffer_load_b128(wsr, oo, 0, 16);
      r1 = __builtin_amdgcn_raw_buffer_load_b128(wsr, oo + 16, 0, 16);
      rm = __builtin_amdgcn_raw_buffer_load_b128(wsr, mo, 0, 16);
      const bool ok = (r0.y == tag) & (r0.w == tag) & (r1.y == tag) & (r1.w == tag) & (rm.y == tag) & (rm.w == tag);
      if (__all(ok) || spins > SPIN_CAP) break;
      __builtin_amdgcn_s_sleep(1);
    }
    const float mi = __uint_as_float(rm.x), li = __uint_as_float(rm.z);
    const float M = max32(mi);
    const float wi = __builtin_amdgcn_exp2f(mi - M);
    const float L = sum32(wi * li);
    const float ci = wi / L;
    const float c = __shfl(ci, pi);
    float o0 = __uint_as_float(r0.x) * c, o1 = __uint_as_float(r0.z) * c, o2 = __uint_as_float(r1.x) * c, o3 = __uint_as_float(r1.z) * c;
    o0 = tr16(o0, o0); o1 = tr16(o1, o1); o2 = tr16(o2, o2); o3 = tr16(o3, o3);
    o0 = tr32(o0, o0); o1 = tr32(o1, o1); o2 = tr32(o2, o2); o3 = tr32(o3, o3);
    if (lane < 16) *(f32x4*)(lds + L_R + w * 64 + lane * 4) = f32x4{o0, o1, o2, o3};
  }
  BAR();
  {
    const int r0 = 8 * w + (lane >> 4);
    float oa = 0.f, ob = 0.f;
#pragma unroll
    for (int i = 0; i < 8; ++i) { oa += lds[L_R + i * 64 + r0]; ob += lds[L_R + i * 64 + r0 + 4]; }
    oa = rbf(oa); ob = rbf(ob);
    float a[8];
#pragma unroll
    for (int i = 0; i < 4; ++i) {
      a[2 * i] = bf_lo(wor[0][i]) * oa + bf_lo(wor[1][i]) * ob;
      a[2 * i + 1] = bf_hi(wor[0][i]) * oa + bf_hi(wor[1][i]) * ob;
    }
    float c4[4], c2[2];
#pragma unroll
    for (int i = 0; i < 4; ++i) c4[i] = tr32(a[i], a[i + 4]);
#pragma unroll
    for (int i = 0; i < 2; ++i) c2[i] = tr16(c4[i], c4[i + 2]);
    const int e = ((lane >> 5) & 1) * 4 + ((lane >> 4) & 1) * 2;
    *(float2*)(lds + L_T + w * 128 + (lane & 15) * 8 + e) = make_float2(c2[0], c2[1]);
  }
  BAR();
  if (tid < 56) {
    float s0 = 0.f, s1 = 0.f;
#pragma unroll
    for (int i = 0; i < 8; ++i) { const float2 t = *(const float2*)(lds + L_T + i * 128 + tid * 2); s0 += t.x; s1 += t.y; }
    const u32x4 gv = {__float_as_uint(s0), tag, __float_as_uint(s1), tag};
    __builtin_amdgcn_raw_buffer_store_b128(gv, wsr, WS_P3 + (unsigned)(((cq * 56 + tid) * 64 + g * 8 + rblk) * 16), 0, 16);
  }
  if (w == 7 && b < 224) {
    u32x4 r;
    for (unsigned spins = 0;; ++spins) {
      asm volatile("" ::: "memory");
      r = __builtin_amdgcn_raw_buffer_load_b128(wsr, WS_P3 + (unsigned)((b * 64 + lane) * 16), 0, 16);
      const bool ok = (r.y == tag) & (r.w == tag);
      if (__all(ok) || spins > SPIN_CAP) break;
      __builtin_amdgcn_s_sleep(1);
    }
    const float a0 = rbf(sum64(__uint_as_float(r.x))), a1 = rbf(sum64(__uint_as_float(r.z)));
    if (lane == 0) {
      float y0 = 0.f, y1 = 0.f;
#pragma unroll
      for (int i = 0; i < 8; ++i) { y0 += lds[L_MLP + 2 * i]; y1 += lds[L_MLP + 2 * i + 1]; }
      const float b2 = bf_lo(p.b2[0]);
      y0 = rbf(rbf(y0) + b2); y1 = rbf(rbf(y1) + b2);
      const unsigned xx = *(const unsigned*)(p.x + d0);
      const float r0 = rbf(rbf(bf_lo(xx) + a0) + y0), r1 = rbf(rbf(bf_hi(xx) + a1) + y1);
      *(unsigned*)(p.out + d0) = pack_bf(r0, r1);
      if (b == 0) __hip_atomic_store((gu32*)(p.ws + WS_EPOCH), tag, RLX_AGENT);
    }
  }
}
}

extern "C" void kernel_launch(void* const* d_in, const int* in_sizes, int n_in,
                              void* d_out, int out_size, void* d_ws, size_t ws_size,
                              hipStream_t stream) {
  Params p;
  p.x = (const unsigned short*)d_in[0]; p.k = (const unsigned short*)d_in[1]; p.v = (const unsigned short*)d_in[2];
  p.wq = (const unsigned short*)d_in[3]; p.wo = (const unsigned short*)d_in[4];
  p.w1 = (const unsigned short*)d_in[6]; p.b1 = (const unsigned short*)d_in[7];
  p.w2 = (const unsigned short*)d_in[8]; p.b2 = (const unsigned short*)d_in[9];
  p.out = (unsigned short*)d_out; p.ws = (unsigned char*)d_ws;
  hipLaunchKernelGGL(fused_decode, dim3(NWG), dim3(NTHR), 0, stream, p);
}
```

```cpp
#include <hip/hip_runtime.h>
#include <hip/hip_bf16.h>
#include <stdint.h>

typedef unsigned u32x4 __attribute__((ext_vector_type(4)));
typedef unsigned u32x2 __attribute__((ext_vector_type(2)));
typedef float f32x4 __attribute__((ext_vector_type(4)));
typedef __bf16 bf16x2 __attribute__((ext_vector_type(2)));
typedef __bf16 bf16x4 __attribute__((ext_vector_type(4)));
typedef __bf16 bf16x8 __attribute__((ext_vector_type(8)));
#define GAS __attribute__((address_space(1)))
#define LAS __attribute__((address_space(3)))
typedef GAS unsigned long long gu64;
typedef GAS unsigned gu32;
#define RLX_AGENT __ATOMIC_RELAXED, __HIP_MEMORY_SCOPE_AGENT
#define RLX_WG __ATOMIC_RELAXED, __HIP_MEMORY_SCOPE_WORKGROUP

namespace {
constexpr int DM = 448, SK = 32768, DH = 256, WIN = 4096;
constexpr int NWG = 256, NTHR = 1024;
constexpr unsigned WS_EPOCH = 0;
constexpr unsigned WS_Q1 = 4096;
constexpr unsigned WS_ML = WS_Q1 + 8 * 256 * 8;
constexpr unsigned WS_O2 = WS_ML + 8 * 32 * 4 * 8;
constexpr unsigned WS_P3 = WS_O2 + 8 * 32 * 512 * 8;
constexpr unsigned WS_MB = WS_P3 + 256 * 448 * 8;
constexpr unsigned WS_END = WS_MB + 448 * 8;
constexpr unsigned SPIN_CAP = 1u << 20;
#define LW 32
#define KPRE 4
constexpr float LOG2E = 1.4426950408889634f;

constexpr int L_K = 0;
constexpr int L_V = 16384;
constexpr int L_CTL = 32768;
constexpr int L_X = L_CTL + 16;
constexpr int L_QP = L_X + 7 * 448;
constexpr int L_QS = L_QP + 7 * 16;
constexpr int L_S = L_QS + 256;
constexpr int L_P = L_S + 256;
constexpr int L_MLP = L_P + 128;
constexpr int L_R = L_MLP + 16;
constexpr int L_F = L_R + 64;
constexpr int L_END = L_F + 128;
constexpr int L_T = L_V;

__device__ __forceinline__ float bf_lo(unsigned w) { return __builtin_bit_cast(float, w << 16); }
__device__ __forceinline__ float bf_hi(unsigned w) { return __builtin_bit_cast(float, w & 0xffff0000u); }
__device__ __forceinline__ float rbf(float f) { return (float)(__bf16)f; }
__device__ __forceinline__ unsigned pack_bf(float lo, float hi) {
  bf16x2 v; v.x = (__bf16)lo; v.y = (__bf16)hi; return __builtin_bit_cast(unsigned, v);
}
template <int CTRL> __device__ __forceinline__ float dpp(float x) {
  return __builtin_bit_cast(float, __builtin_amdgcn_mov_dpp(__builtin_bit_cast(int, x), CTRL, 0xf, 0xf, true));
}
constexpr int XOR1 = 0xB1, XOR2 = 0x4E, XOR7 = 0x141, XOR8 = 0x128;
__device__ __forceinline__ float tr16(float lo, float hi) {
  auto s = __builtin_amdgcn_permlane16_swap(__float_as_uint(lo), __float_as_uint(hi), false, false);
  return __uint_as_float(s[0]) + __uint_as_float(s[1]);
}
__device__ __forceinline__ float tr32(float lo, float hi) {
  auto s = __builtin_amdgcn_permlane32_swap(__float_as_uint(lo), __float_as_uint(hi), false, false);
  return __uint_as_float(s[0]) + __uint_as_float(s[1]);
}
template <int CTRL> __device__ __forceinline__ float trd(float lo, float hi, bool bit) {
  const float keep = bit ? hi : lo, send = bit ? lo : hi;
  return keep + dpp<CTRL>(send);
}
__device__ __forceinline__ float sum16(float v) {
  v += dpp<XOR1>(v); v += dpp<XOR2>(v); v += dpp<XOR7>(v); v += dpp<XOR8>(v);
  return v;
}
__device__ __forceinline__ float sum32(float v) { v = sum16(v); return tr16(v, v); }
__device__ __forceinline__ float sum64(float v) { v = sum32(v); return tr32(v, v); }
__device__ __forceinline__ float max32(float v) {
  v = fmaxf(v, dpp<XOR1>(v)); v = fmaxf(v, dpp<XOR2>(v)); v = fmaxf(v, dpp<XOR7>(v)); v = fmaxf(v, dpp<XOR8>(v));
  auto s = __builtin_amdgcn_permlane16_swap(__float_as_uint(v), __float_as_uint(v), false, false);
  return fmaxf(__uint_as_float(s[0]), __uint_as_float(s[1]));
}
__device__ __forceinline__ u32x4 ntload(const void* p) { return __builtin_nontemporal_load((const u32x4*)p); }
#define LGKM0() asm volatile("s_waitcnt lgkmcnt(0)" ::: "memory")
#define BAR() do { asm volatile("s_waitcnt lgkmcnt(0)" ::: "memory"); __builtin_amdgcn_s_barrier(); asm volatile("" ::: "memory"); } while (0)
__device__ __forceinline__ void lds_wait_ge(LAS unsigned* w, unsigned v) {
  for (unsigned spins = 0; __hip_atomic_load(w, RLX_WG) < v && spins < (SPIN_CAP << 4); ++spins) __builtin_amdgcn_s_sleep(1);
  asm volatile("" ::: "memory");
}
__device__ __forceinline__ void lds_put(LAS unsigned* w, unsigned v) {
  asm volatile("ds_write_b32 %0, %1" :: "v"((unsigned)(uintptr_t)w), "v"(v) : "memory");
}
__device__ __forceinline__ unsigned lds_get(LAS unsigned* w) {
  unsigned r; asm volatile("ds_read_b32 %0, %1\n\ts_waitcnt lgkmcnt(0)" : "=v"(r) : "v"((unsigned)(uintptr_t)w) : "memory"); return r;
}

#define STAMP(i) do { } while (0)
#define LSTAMP(i) do { } while (0)
#define STAMP_DUMP() do { } while (0)

struct Params {
  const unsigned short* x; const unsigned short* k; const unsigned short* v; const unsigned short* wq; const unsigned short* wo;
  const unsigned short* w1; const unsigned short* b1; const unsigned short* w2; const unsigned short* b2;
  unsigned short* out; unsigned char* ws;
};

__device__ __forceinline__ void ln_stats(const float (&xf)[8], float lv, float& mb, float& rd) {
  float s = 0.f;
#pragma unroll
  for (int i = 0; i < 8; ++i) s += xf[i];
  const float mean = sum64(s * lv) * (1.f / DM);
  float q2 = 0.f;
#pragma unroll
  for (int i = 0; i < 8; ++i) { const float d = xf[i] - mean; q2 += d * d; }
  const float var = sum64(q2 * lv) * (1.f / DM);
  mb = rbf(mean); rd = 1.f / rbf(sqrtf(rbf(rbf(var) + 1e-5f)));
}

__global__ void __launch_bounds__(NTHR, 4) fused_decode(Params p) {
  __shared__ __attribute__((aligned(16))) float lds[L_END + 64];
  const int b = blockIdx.x, g = b & 7, j = b >> 3;
  const int tid = threadIdx.x, lane = tid & 63, w = __builtin_amdgcn_readfirstlane(tid >> 6);
  const __amdgpu_buffer_rsrc_t wsr = __builtin_amdgcn_make_buffer_rsrc(p.ws, 0, WS_END, 0x00020000);
  gu64* const wsg = (gu64*)p.ws;
  LAS unsigned* const ctl = (LAS unsigned*)(lds + L_CTL);
  if (w == 0) STAMP(0);
  if (tid < 8) ctl[tid] = 0u;
  unsigned eoff = WS_EPOCH; asm volatile("" : "+v"(eoff));
  unsigned tagraw = 0u;
  if (w != 0) tagraw = __builtin_amdgcn_raw_buffer_load_b32(wsr, eoff, 0, 16);
#define tag (tagraw + 1u)
  u32x4 wor = {0u, 0u, 0u, 0u};
  const unsigned short* const wop = p.wo + (size_t)(g * 512 + j * 16 + w) * DM + (lane < 56 ? lane : 55) * 8;
  if (w != 0) wor = ntload(wop);

  if (w == 0) {
    const size_t kvo = ((size_t)g * SK + (SK - WIN) + j * 128) * DH;
    int ko[8], vo[8];
#pragma unroll
    for (int i = 0; i < 8; ++i) {
      const int row = 2 * i + (lane >> 5), f = (row & 3) | (((row >> 3) & 1) << 2);
      ko[i] = row * 256 + (((lane & 31) ^ row) * 8);
      vo[i] = row * 256 + (((lane & 31) ^ (f << 1)) * 8);
    }
    BAR();
#pragma unroll 1
    for (int c = 0; c < 16; ++c) {
      const unsigned short* src = (c < 8 ? p.k : p.v) + kvo + (c & 7) * 4096;
      if (c == KPRE)
        for (unsigned spins = 0; __builtin_amdgcn_readfirstlane(lds_get(ctl + 2)) == 0u && spins < (SPIN_CAP << 4); ++spins) __builtin_amdgcn_s_sleep(1);
#pragma unroll
      for (int i = 0; i < 8; ++i)
        __builtin_amdgcn_global_load_lds((const unsigned*)(src + (c < 8 ? ko[i] : vo[i])), (LAS unsigned*)(lds + (c * 8 + i) * 256), 16, 0, 2);
      if (8 * (c + 1) >= LW) {
        asm volatile("s_waitcnt vmcnt(%0)" :: "n"(LW - 8) : "memory");
        lds_put(ctl + 3, (unsigned)(c + 2 - LW / 8));
        if (c + 2 - LW / 8 == 8) LSTAMP(4);
      }
    }
    LSTAMP(9);
    tagraw = __builtin_amdgcn_raw_buffer_load_b32(wsr, eoff, 0, 16);
    wor = ntload(wop);
#pragma unroll
    for (int t = LW / 8 - 2; t >= 0; --t) {
      asm volatile("s_waitcnt vmcnt(%0)" :: "n"(8 * t + 2) : "memory");
      lds_put(ctl + 3, (unsigned)(16 - t));
      if (16 - t == 8) LSTAMP(4);
    }
    LSTAMP(10);
  } else if (w < 8) {
    const int u = w - 1;
    const u32x4 xr = *(const u32x4*)(p.x + (lane < 56 ? lane : 55) * 8);
    const unsigned short* s = p.wq + (size_t)(u * 64 + (lane & 31)) * 4096 + g * 512 + j * 16 + (lane >> 5) * 8;
    const u32x4 wq0 = ntload(s), wq1 = ntload(s + 32 * 4096);
    const int fo = (u * 64 + lane) * 4;
    const u32x2 m1r = *(const u32x2*)(p.w1 + fo), mbr = *(const u32x2*)(p.b1 + fo), m2r = *(const u32x2*)(p.w2 + fo);
    __builtin_amdgcn_sched_barrier(0);
    BAR();
    const float lv = lane < 56 ? 1.f : 0.f;
    float* const xl = lds + L_X + u * 448;
    float xf[8], mb, rd;
#pragma unroll
    for (int i = 0; i < 4; ++i) { xf[2 * i] = bf_lo(xr[i]); xf[2 * i + 1] = bf_hi(xr[i]); }
    ln_stats(xf, lv, mb, rd);
#pragma unroll
    for (int i = 0; i < 8; ++i) xf[i] = rbf(rbf(xf[i] - mb) * rd);
    if (lane < 56) {
      *(f32x4*)(xl + lane * 8) = f32x4{xf[0], xf[1], xf[2], xf[3]};
      *(f32x4*)(xl + lane * 8 + 4) = f32x4{xf[4], xf[5], xf[6], xf[7]};
    }
    LGKM0();
    {
      const float x0 = xl[u * 64 + (lane & 31)], x1 = xl[u * 64 + 32 + (lane & 31)];
      float a[8];
#pragma unroll
      for (int i = 0; i < 4; ++i) {
        a[2 * i] = bf_lo(wq0[i]) * x0 + bf_lo(wq1[i]) * x1;
        a[2 * i + 1] = bf_hi(wq0[i]) * x0 + bf_hi(wq1[i]) * x1;
      }
      float c4[4], c2[2];
#pragma unroll
      for (int i = 0; i < 4; ++i) c4[i] = tr16(a[i], a[i + 4]);
#pragma unroll
      for (int i = 0; i < 2; ++i) c2[i] = trd<XOR8>(c4[i], c4[i + 2], (lane & 8) != 0);
      float c1 = trd<XOR7>(c2[0], c2[1], (lane & 4) != 0);
      c1 += dpp<XOR2>(c1); c1 += dpp<XOR1>(c1);
      const int col = (lane >> 5) * 8 + ((lane >> 4) & 1) * 4 + ((lane >> 3) & 1) * 2 + ((lane >> 2) & 1);
      if ((lane & 3) == 0) lds[L_QP + u * 16 + col] = c1;
      LGKM0();
      unsigned old = 0u;
      if (lane == 0) old = __hip_atomic_fetch_add(ctl, 1u, RLX_WG);
      old = __builtin_amdgcn_readfirstlane(old);
      asm volatile("" ::: "memory");
      if (old == 6u) {
        float sm = 0.f;
#pragma unroll
        for (int i = 0; i < 7; ++i) sm += lds[L_QP + i * 16 + (lane & 15)];
        const float sn = dpp<XOR1>(sm);
        const unsigned pr = pack_bf(sm, sn);
        const unsigned pr2 = __builtin_bit_cast(unsigned, dpp<XOR2>(__builtin_bit_cast(float, pr)));
        if (lane < 16 && (lane & 3) == 0) {
          const u32x4 gv = {pr, tag, pr2, tag};
          __builtin_amdgcn_raw_buffer_store_b128(gv, wsr, WS_Q1 + (unsigned)((g * 256 + j * 8 + (lane >> 1)) * 8), 0, 16);
        }
        if (lane == 0) __hip_atomic_store(ctl + 2, 1u, RLX_WG);
        STAMP(1);
      }
    }
    {
      float mb2, rd2;
      ln_stats(xf, lv, mb2, rd2);
      const int dd = b < 224 ? 2 * b : 0;
      const float h0 = rbf(rbf(xl[dd] - mb2) * rd2), h1 = rbf(rbf(xl[dd + 1] - mb2) * rd2);
      float y0 = 0.f, y1 = 0.f;
#pragma unroll
      for (int i = 0; i < 4; ++i) {
        const float a1 = (i & 1) ? bf_hi(m1r[i >> 1]) : bf_lo(m1r[i >> 1]);
        const float ab = (i & 1) ? bf_hi(mbr[i >> 1]) : bf_lo(mbr[i >> 1]);
        const float a2 = (i & 1) ? bf_hi(m2r[i >> 1]) : bf_lo(m2r[i >> 1]);
        const float u0 = rbf(rbf(h0 * a1) + ab), u1 = rbf(rbf(h1 * a1) + ab);
        const float z0 = 0.7978845608028654f * (u0 + 0.044715f * u0 * u0 * u0);
        const float z1 = 0.7978845608028654f * (u1 + 0.044715f * u1 * u1 * u1);
        const float g0 = rbf(u0 * __builtin_amdgcn_rcpf(1.f + __expf(-2.f * z0)));
        const float g1 = rbf(u1 * __builtin_amdgcn_rcpf(1.f + __expf(-2.f * z1)));
        y0 += g0 * a2; y1 += g1 * a2;
      }
      y0 = sum64(y0); y1 = sum64(y1);
      if (lane == 0) { lds[L_MLP + 2 * u] = y0; lds[L_MLP + 2 * u + 1] = y1; }
      LGKM0();
      unsigned old = 0u;
      if (lane == 0) old = __hip_atomic_fetch_add(ctl + 6, 1u, RLX_WG);
      old = __builtin_amdgcn_readfirstlane(old);
      asm volatile("" ::: "memory");
      if (old == 6u && b < 224 && lane == 0) {
        float s0 = 0.f, s1 = 0.f;
#pragma unroll
        for (int i = 0; i < 7; ++i) { s0 += lds[L_MLP + 2 * i]; s1 += lds[L_MLP + 2 * i + 1]; }
        const float b2 = bf_lo(p.b2[0]);
        s0 = rbf(rbf(s0) + b2); s1 = rbf(rbf(s1) + b2);
        const u32x4 gv = {__float_as_uint(s0), tag, __float_as_uint(s1), tag};
        __builtin_amdgcn_raw_buffer_store_b128(gv, wsr, WS_MB + (unsigned)(b * 16), 0, 16);
        STAMP(11);
      }
    }
  } else {
    const int u = w - 8;
    __builtin_amdgcn_sched_barrier(0);
    BAR();
    if (u == 0) {
      u32x4 r0, r1;
      for (unsigned spins = 0;; ++spins) {
        asm volatile("" ::: "memory");
        r0 = __builtin_amdgcn_raw_buffer_load_b128(wsr, WS_Q1 + (unsigned)((g * 256 + 2 * lane) * 8), 0, 16);
        r1 = __builtin_amdgcn_raw_buffer_load_b128(wsr, WS_Q1 + (unsigned)((g * 256 + 128 + 2 * lane) * 8), 0, 16);
        if (__all((r0.y == tag) & (r0.w == tag) & (r1.y == tag) & (r1.w == tag)) || spins > SPIN_CAP) break;
        __builtin_amdgcn_s_sleep(1);
      }
      STAMP(2);
      *(u32x2*)(lds + L_QS + 2 * lane) = u32x2{r0.x, r0.z};
      *(u32x2*)(lds + L_QS + 128 + 2 * lane) = u32x2{r1.x, r1.z};
      LGKM0();
      if (lane == 0) __hip_atomic_store(ctl + 1, 1u, RLX_WG);
    } else {
      lds_wait_ge(ctl + 1, 1u);
    }
    lds_wait_ge(ctl + 3, (unsigned)(u + 1));
    {
      const int r = lane & 15, gq = lane >> 4;
      const bool qrow = r < 2;
      const float* qp = lds + L_QS + (qrow ? r : 0) * 128 + gq * 4;
      const char* kbase = (const char*)(lds + L_K) + (u * 16 + r) * 512;
      f32x4 acc = {0.f, 0.f, 0.f, 0.f};
#pragma unroll
      for (int s = 0; s < 8; ++s) {
        u32x4 qa = *(const u32x4*)(qp + s * 16);
        if (!qrow) qa = u32x4{0u, 0u, 0u, 0u};
        const int ch = 4 * s + gq;
        const u32x4 kb = *(const u32x4*)(kbase + ((ch & 16) | ((ch ^ r) & 15)) * 16);
        acc = __builtin_amdgcn_mfma_f32_16x16x32_bf16(__builtin_bit_cast(bf16x8, qa), __builtin_bit_cast(bf16x8, kb), acc, 0, 0, 0);
      }
      if (lane < 16) {
        lds[L_S + u * 16 + lane] = rbf(acc[0]) * (0.0625f * LOG2E);
        lds[L_S + 128 + u * 16 + lane] = rbf(acc[1]) * (0.0625f * LOG2E);
      }
    }
    LGKM0();
    unsigned old = 0u;
    if (lane == 0) old = __hip_atomic_fetch_add(ctl + 4, 1u, RLX_WG);
    old = __builtin_amdgcn_readfirstlane(old);
    asm volatile("" ::: "memory");
    if (old == 7u) {
      STAMP(3);
      const f32x4 s4 = *(const f32x4*)(lds + L_S + lane * 4);
      const float mh = max32(fmaxf(fmaxf(s4.x, s4.y), fmaxf(s4.z, s4.w)));
      const float p0 = rbf(__builtin_amdgcn_exp2f(s4.x - mh)), p1 = rbf(__builtin_amdgcn_exp2f(s4.y - mh));
      const float p2 = rbf(__builtin_amdgcn_exp2f(s4.z - mh)), p3 = rbf(__builtin_amdgcn_exp2f(s4.w - mh));
      *(u32x2*)(lds + L_P + lane * 2) = u32x2{pack_bf(p0, p1), pack_bf(p2, p3)};
      LGKM0();
      if (lane == 0) __hip_atomic_store(ctl + 5, 1u, RLX_WG);
      STAMP(12);
      const float e = sum32((p0 + p1) + (p2 + p3));
      if ((lane & 31) == 0) {
        const u32x4 gv = {__float_as_uint(mh), tag, __float_as_uint(e), tag};
        __builtin_amdgcn_raw_buffer_store_b128(gv, wsr, WS_ML + (unsigned)(((g * 32 + j) * 2 + (lane >> 5)) * 16), 0, 16);
      }
    }
  }
  {
    lds_wait_ge(ctl + 5, 1u);
    const int r = lane & 15, gq = lane >> 4, q = r >> 2, pc = lane & 3;
    const bool prow = r < 2;
    const float* pp = lds + L_P + (prow ? r : 0) * 64 + gq * 4;
    f32x4 acc = {0.f, 0.f, 0.f, 0.f};
#pragma unroll
    for (int s = 0; s < 4; ++s) {
      u32x4 pa = *(const u32x4*)(pp + s * 16);
      if (!prow) pa = u32x4{0u, 0u, 0u, 0u};
      lds_wait_ge(ctl + 3, (unsigned)(10 + 2 * s));
      const int f = q | ((gq & 1) << 2);
      const int ch = (2 * w + (pc >> 1)) ^ (f << 1);
      const char* vb = (const char*)(lds + L_V) + (32 * s + 8 * gq + q) * 512 + ch * 16 + (pc & 1) * 8;
      const bf16x4 v0 = __builtin_amdgcn_ds_read_tr16_b64_v4bf16((LAS bf16x4*)(vb));
      const bf16x4 v1 = __builtin_amdgcn_ds_read_tr16_b64_v4bf16((LAS bf16x4*)(vb + 4 * 512));
      const bf16x8 vv = {v0[0], v0[1], v0[2], v0[3], v1[0], v1[1], v1[2], v1[3]};
      acc = __builtin_amdgcn_mfma_f32_16x16x32_bf16(__builtin_bit_cast(bf16x8, pa), vv, acc, 0, 0, 0);
    }
    const float an = dpp<XOR1>(acc[0]), bn = dpp<XOR1>(acc[1]);
    if (lane < 16) {
      const bool odd = lane & 1;
      const u32x4 gv = {__float_as_uint(odd ? bn : acc[0]), tag, __float_as_uint(odd ? acc[1] : an), tag};
      __builtin_amdgcn_raw_buffer_store_b128(gv, wsr, WS_O2 + (unsigned)(((g * 32 + j) * 512 + (odd ? 256 : 0) + 16 * w + (lane & 14)) * 8), 0, 16);
    }
    if (w == 9) STAMP(5);
  }
  if (w >= 4 && w < 8) {
    const int pi = 8 * (w - 4) + (lane >> 3);
    const unsigned oo = WS_O2 + (unsigned)(((g * 32 + pi) * 512 + j * 16 + (lane & 7) * 2) * 8);
    const unsigned mo = WS_ML + (unsigned)(((g * 32 + (lane & 31)) * 2 + (j >> 4)) * 16);
    u32x4 r0, rm;
    for (unsigned spins = 0;; ++spins) {
      asm volatile("" ::: "memory");
      r0 = __builtin_amdgcn_raw_buffer_load_b128(wsr, oo, 0, 16);
      rm = __builtin_amdgcn_raw_buffer_load_b128(wsr, mo, 0, 16);
      const bool ok = (r0.y == tag) & (r0.w == tag) & (rm.y == tag) & (rm.w == tag);
      if (__all(ok) || spins > SPIN_CAP) break;
      __builtin_amdgcn_s_sleep(1);
    }
    const float mi = __uint_as_float(rm.x), li = __uint_as_float(rm.z);
    const float M = max32(mi);
    const float wi = __builtin_amdgcn_exp2f(mi - M);
    const float L = sum32(wi * li);
    const float ci = wi / L;
    const float c = __shfl(ci, pi);
    float o0 = __uint_as_float(r0.x) * c, o1 = __uint_as_float(r0.z) * c;
    o0 += dpp<XOR8>(o0); o1 += dpp<XOR8>(o1);
    o0 = tr16(o0, o0); o1 = tr16(o1, o1);
    o0 = tr32(o0, o0); o1 = tr32(o1, o1);
    if (lane < 8) *(float2*)(lds + L_R + (w - 4) * 16 + lane * 2) = make_float2(o0, o1);
  }
  BAR();
  if (w == 0) STAMP(6);
  {
    const float ov = rbf((lds[L_R + w] + lds[L_R + 16 + w]) + (lds[L_R + 32 + w] + lds[L_R + 48 + w]));
    if (lane < 56) {
      float* t = lds + L_T + w * 448 + lane * 8;
      *(f32x4*)(t) = f32x4{bf_lo(wor[0]) * ov, bf_hi(wor[0]) * ov, bf_lo(wor[1]) * ov, bf_hi(wor[1]) * ov};
      *(f32x4*)(t + 4) = f32x4{bf_lo(wor[2]) * ov, bf_hi(wor[2]) * ov, bf_lo(wor[3]) * ov, bf_hi(wor[3]) * ov};
    }
  }
  BAR();
  if (tid < 224) {
    float s0 = 0.f, s1 = 0.f;
#pragma unroll
    for (int i = 0; i < 16; ++i) { const float2 t = *(const float2*)(lds + L_T + i * 448 + tid * 2); s0 += t.x; s1 += t.y; }
    const u32x4 gv = {__float_as_uint(s0), tag, __float_as_uint(s1), tag};
    __builtin_amdgcn_raw_buffer_store_b128(gv, wsr, WS_P3 + (unsigned)((b * 224 + tid) * 16), 0, 16);
  }
  if (w == 0) STAMP(7);
  if (b < 56) {
    u32x4 r, rb;
    const unsigned xx = *(const unsigned*)(p.x + 8 * b + 2 * (lane & 3));
    for (unsigned spins = 0;; ++spins) {
      asm volatile("" ::: "memory");
      r = __builtin_amdgcn_raw_buffer_load_b128(wsr, WS_P3 + (unsigned)(((tid >> 2) * 224 + 4 * b + (tid & 3)) * 16), 0, 16);
      rb = __builtin_amdgcn_raw_buffer_load_b128(wsr, WS_MB + (unsigned)((4 * b + (tid & 3)) * 16), 0, 16);
      const bool ok = (r.y == tag) & (r.w == tag) & (rb.y == tag) & (rb.w == tag);
      if (__all(ok) || spins > SPIN_CAP) break;
      __builtin_amdgcn_s_sleep(1);
    }
    float s0 = __uint_as_float(r.x), s1 = __uint_as_float(r.z);
    s0 += dpp<XOR7>(dpp<XOR1>(dpp<XOR2>(s0))); s1 += dpp<XOR7>(dpp<XOR1>(dpp<XOR2>(s1)));
    s0 += dpp<XOR8>(s0); s1 += dpp<XOR8>(s1);
    s0 = tr16(s0, s0); s1 = tr16(s1, s1);
    s0 = tr32(s0, s0); s1 = tr32(s1, s1);
    if (lane < 4) *(float2*)(lds + L_F + w * 8 + lane * 2) = make_float2(s0, s1);
    BAR();
    if (tid < 4) {
      float a0 = 0.f, a1 = 0.f;
#pragma unroll
      for (int i = 0; i < 16; ++i) { const float2 t = *(const float2*)(lds + L_F + i * 8 + tid * 2); a0 += t.x; a1 += t.y; }
      const float r0 = rbf(rbf(bf_lo(xx) + rbf(a0)) + __uint_as_float(rb.x)), r1 = rbf(rbf(bf_hi(xx) + rbf(a1)) + __uint_as_float(rb.z));
      *(unsigned*)(p.out + 8 * b + 2 * tid) = pack_bf(r0, r1);
      if (b == 0 && tid == 0) __hip_atomic_store((gu32*)(p.ws + WS_EPOCH), tag, RLX_AGENT);
    }
    if (w == 0) STAMP(8);
  }
}
}

extern "C" void kernel_launch(void* const* d_in, const int* in_sizes, int n_in,
                              void* d_out, int out_size, void* d_ws, size_t ws_size,
                              hipStream_t stream) {
  Params p;
  p.x = (const unsigned short*)d_in[0]; p.k = (const unsigned short*)d_in[1]; p.v = (const unsigned short*)d_in[2];
  p.wq = (const unsigned short*)d_in[3]; p.wo = (const unsigned short*)d_in[4];
  p.w1 = (const unsigned short*)d_in[6]; p.b1 = (const unsigned short*)d_in[7];
  p.w2 = (const unsigned short*)d_in[8]; p.b2 = (const unsigned short*)d_in[9];
  p.out = (unsigned short*)d_out; p.ws = (unsigned char*)d_ws;
  hipLaunchKernelGGL(fused_decode, dim3(NWG), dim3(NTHR), 0, stream, p);
}
```

```cpp
#include <hip/hip_runtime.h>
#include <hip/hip_bf16.h>
#include <stdint.h>

typedef unsigned u32x4 __attribute__((ext_vector_type(4)));
typedef unsigned u32x2 __attribute__((ext_vector_type(2)));
typedef float f32x4 __attribute__((ext_vector_type(4)));
typedef __bf16 bf16x2 __attribute__((ext_vector_type(2)));
typedef __bf16 bf16x4 __attribute__((ext_vector_type(4)));
typedef __bf16 bf16x8 __attribute__((ext_vector_type(8)));
#define GAS __attribute__((address_space(1)))
#define LAS __attribute__((address_space(3)))
typedef GAS unsigned long long gu64;
typedef GAS unsigned gu32;
#define RLX_AGENT __ATOMIC_RELAXED, __HIP_MEMORY_SCOPE_AGENT
#define RLX_WG __ATOMIC_RELAXED, __HIP_MEMORY_SCOPE_WORKGROUP

namespace {
constexpr int DM = 448, SK = 32768, DH = 256, WIN = 4096;
constexpr int NWG = 256, NTHR = 1024;
constexpr unsigned WS_EPOCH = 0;
constexpr unsigned WS_Q1 = 4096;
constexpr unsigned WS_ML = WS_Q1 + 8 * 256 * 8;
constexpr unsigned WS_O2 = WS_ML + 8 * 32 * 4 * 8;
constexpr unsigned WS_P3 = WS_O2 + 8 * 32 * 512 * 8;
constexpr unsigned WS_MB = WS_P3 + 256 * 448 * 8;
constexpr unsigned WS_END = WS_MB + 448 * 8;
constexpr unsigned SPIN_CAP = 1u << 20;
#define LW 32
#define KPRE 4
constexpr float LOG2E = 1.4426950408889634f;

constexpr int L_K = 0;
constexpr int L_V = 16384;
constexpr int L_CTL = 32768;
constexpr int L_X = L_CTL + 16;
constexpr int L_QP = L_X + 7 * 448;
constexpr int L_QS = L_QP + 7 * 16;
constexpr int L_S = L_QS + 256;
constexpr int L_P = L_S + 256;
constexpr int L_MLP = L_P + 128;
constexpr int L_R = L_MLP + 16;
constexpr int L_F = L_R + 64;
constexpr int L_END = L_F + 128;
constexpr int L_T = L_V;

__device__ __forceinline__ float bf_lo(unsigned w) { return __builtin_bit_cast(float, w << 16); }
__device__ __forceinline__ float bf_hi(unsigned w) { return __builtin_bit_cast(float, w & 0xffff0000u); }
__device__ __forceinline__ float rbf(float f) { return (float)(__bf16)f; }
__device__ __forceinline__ unsigned pack_bf(float lo, float hi) {
  bf16x2 v; v.x = (__bf16)lo; v.y = (__bf16)hi; return __builtin_bit_cast(unsigned, v);
}
template <int CTRL> __device__ __forceinline__ float dpp(float x) {
  return __builtin_bit_cast(float, __builtin_amdgcn_mov_dpp(__builtin_bit_cast(int, x), CTRL, 0xf, 0xf, true));
}
constexpr int XOR1 = 0xB1, XOR2 = 0x4E, XOR7 = 0x141, XOR8 = 0x128;
__device__ __forceinline__ float tr16(float lo, float hi) {
  auto s = __builtin_amdgcn_permlane16_swap(__float_as_uint(lo), __float_as_uint(hi), false, false);
  return __uint_as_float(s[0]) + __uint_as_float(s[1]);
}
__device__ __forceinline__ float tr32(float lo, float hi) {
  auto s = __builtin_amdgcn_permlane32_swap(__float_as_uint(lo), __float_as_uint(hi), false, false);
  return __uint_as_float(s[0]) + __uint_as_float(s[1]);
}
template <int CTRL> __device__ __forceinline__ float trd(float lo, float hi, bool bit) {
  const float keep = bit ? hi : lo, send = bit ? lo : hi;
  return keep + dpp<CTRL>(send);
}
__device__ __forceinline__ float sum16(float v) {
  v += dpp<XOR1>(v); v += dpp<XOR2>(v); v += dpp<XOR7>(v); v += dpp<XOR8>(v);
  return v;
}
__device__ __forceinline__ float sum32(float v) { v = sum16(v); return tr16(v, v); }
__device__ __forceinline__ float sum64(float v) { v = sum32(v); return tr32(v, v); }
__device__ __forceinline__ float max32(float v) {
  v = fmaxf(v, dpp<XOR1>(v)); v = fmaxf(v, dpp<XOR2>(v)); v = fmaxf(v, dpp<XOR7>(v)); v = fmaxf(v, dpp<XOR8>(v));
  auto s = __builtin_amdgcn_permlane16_swap(__float_as_uint(v), __float_as_uint(v), false, false);
  return fmaxf(__uint_as_float(s[0]), __uint_as_float(s[1]));
}
__device__ __forceinline__ u32x4 ntload(const void* p) { return __builtin_nontemporal_load((const u32x4*)p); }
#define LGKM0() asm volatile("s_waitcnt lgkmcnt(0)" ::: "memory")
#define BAR() do { asm volatile("s_waitcnt lgkmcnt(0)" ::: "memory"); __builtin_amdgcn_s_barrier(); asm volatile("" ::: "memory"); } while (0)
__device__ __forceinline__ void lds_wait_ge(LAS unsigned* w, unsigned v) {
  for (unsigned spins = 0; __hip_atomic_load(w, RLX_WG) < v && spins < (SPIN_CAP << 4); ++spins) __builtin_amdgcn_s_sleep(1);
  asm volatile("" ::: "memory");
}
__device__ __forceinline__ void lds_put(LAS unsigned* w, unsigned v) {
  asm volatile("ds_write_b32 %0, %1" :: "v"((unsigned)(uintptr_t)w), "v"(v) : "memory");
}
__device__ __forceinline__ unsigned lds_get(LAS unsigned* w) {
  unsigned r; asm volatile("ds_read_b32 %0, %1\n\ts_waitcnt lgkmcnt(0)" : "=v"(r) : "v"((unsigned)(uintptr_t)w) : "memory"); return r;
}

#define STAMP(i) do { } while (0)
#define LSTAMP(i) do { } while (0)
#define STAMP_DUMP() do { } while (0)

struct Params {
  const unsigned short* x; const unsigned short* k; const unsigned short* v; const unsigned short* wq; const unsigned short* wo;
  const unsigned short* w1; const unsigned short* b1; const unsigned short* w2; const unsigned short* b2;
  unsigned short* out; unsigned char* ws;
};

__device__ __forceinline__ void ln_stats(const float (&xf)[8], float lv, float& mb, float& rd) {
  float s = 0.f;
#pragma unroll
  for (int i = 0; i < 8; ++i) s += xf[i];
  const float mean = sum64(s * lv) * (1.f / DM);
  float q2 = 0.f;
#pragma unroll
  for (int i = 0; i < 8; ++i) { const float d = xf[i] - mean; q2 += d * d; }
  const float var = sum64(q2 * lv) * (1.f / DM);
  mb = rbf(mean); rd = 1.f / rbf(sqrtf(rbf(rbf(var) + 1e-5f)));
}

__global__ void __launch_bounds__(NTHR, 4) fused_decode(Params p) {
  __shared__ __attribute__((aligned(16))) float lds[L_END + 64];
  const int b = blockIdx.x, g = b & 7, j = b >> 3;
  const int tid = threadIdx.x, lane = tid & 63, w = __builtin_amdgcn_readfirstlane(tid >> 6);
  const __amdgpu_buffer_rsrc_t wsr = __builtin_amdgcn_make_buffer_rsrc(p.ws, 0, WS_END, 0x00020000);
  gu64* const wsg = (gu64*)p.ws;
  LAS unsigned* const ctl = (LAS unsigned*)(lds + L_CTL);
  if (w == 0) STAMP(0);
  if (tid < 8) ctl[tid] = 0u;
  unsigned eoff = WS_EPOCH; asm volatile("" : "+v"(eoff));
  unsigned tagraw = 0u;
  if (w != 0) tagraw = __builtin_amdgcn_raw_buffer_load_b32(wsr, eoff, 0, 16);
#define tag (tagraw + 1u)
  u32x4 wor = {0u, 0u, 0u, 0u};
  const unsigned short* const wop = p.wo + (size_t)(g * 512 + j * 16 + w) * DM + (lane < 56 ? lane : 55) * 8;
  if (w != 0) wor = ntload(wop);

  if (w == 0) {
    const size_t kvo = ((size_t)g * SK + (SK - WIN) + j * 128) * DH;
    int ko[8], vo[8];
#pragma unroll
    for (int i = 0; i < 8; ++i) {
      const int row = 2 * i + (lane >> 5), f = (row & 3) | (((row >> 3) & 1) << 2);
      ko[i] = row * 256 + (((lane & 31) ^ row) * 8);
      vo[i] = row * 256 + (((lane & 31) ^ (f << 1)) * 8);
    }
    BAR();
#pragma unroll 1
    for (int c = 0; c < 16; ++c) {
      const unsigned short* src = (c < 8 ? p.k : p.v) + kvo + (c & 7) * 4096;
      if (c == KPRE)
        for (unsigned spins = 0; __builtin_amdgcn_readfirstlane(lds_get(ctl + 2)) == 0u && spins < (SPIN_CAP << 4); ++spins) __builtin_amdgcn_s_sleep(1);
#pragma unroll
      for (int i = 0; i < 8; ++i)
        __builtin_amdgcn_global_load_lds((const unsigned*)(src + (c < 8 ? ko[i] : vo[i])), (LAS unsigned*)(lds + (c * 8 + i) * 256), 16, 0, 2);
      if (8 * (c + 1) >= LW) {
        asm volatile("s_waitcnt vmcnt(%0)" :: "n"(LW - 8) : "memory");
        lds_put(ctl + 3, (unsigned)(c + 2 - LW / 8));
        if (c + 2 - LW / 8 == 8) LSTAMP(4);
      }
    }
    LSTAMP(9);
    tagraw = __builtin_amdgcn_raw_buffer_load_b32(wsr, eoff, 0, 16);
    wor = ntload(wop);
#pragma unroll
    for (int t = LW / 8 - 2; t >= 0; --t) {
      asm volatile("s_waitcnt vmcnt(%0)" :: "n"(8 * t + 2) : "memory");
      lds_put(ctl + 3, (unsigned)(16 - t));
      if (16 - t == 8) LSTAMP(4);
    }
    LSTAMP(10);
  } else if (w < 8) {
    const int u = w - 1;
    const u32x4 xr = *(const u32x4*)(p.x + (lane < 56 ? lane : 55) * 8);
    const unsigned short* s = p.wq + (size_t)(u * 64 + (lane & 31)) * 4096 + g * 512 + j * 16 + (lane >> 5) * 8;
    const u32x4 wq0 = ntload(s), wq1 = ntload(s + 32 * 4096);
    const int fo = (u * 64 + lane) * 4;
    const u32x2 m1r = *(const u32x2*)(p.w1 + fo), mbr = *(const u32x2*)(p.b1 + fo), m2r = *(const u32x2*)(p.w2 + fo);
    __builtin_amdgcn_sched_barrier(0);
    BAR();
    const float lv = lane < 56 ? 1.f : 0.f;
    float* const xl = lds + L_X + u * 448;
    float xf[8], mb, rd;
#pragma unroll
    for (int i = 0; i < 4; ++i) { xf[2 * i] = bf_lo(xr[i]); xf[2 * i + 1] = bf_hi(xr[i]); }
    ln_stats(xf, lv, mb, rd);
#pragma unroll
    for (int i = 0; i < 8; ++i) xf[i] = rbf(rbf(xf[i] - mb) * rd);
    if (lane < 56) {
      *(f32x4*)(xl + lane * 8) = f32x4{xf[0], xf[1], xf[2], xf[3]};
      *(f32x4*)(xl + lane * 8 + 4) = f32x4{xf[4], xf[5], xf[6], xf[7]};
    }
    LGKM0();
    {
      const float x0 = xl[u * 64 + (lane & 31)], x1 = xl[u * 64 + 32 + (lane & 31)];
      float a[8];
#pragma unroll
      for (int i = 0; i < 4; ++i) {
        a[2 * i] = bf_lo(wq0[i]) * x0 + bf_lo(wq1[i]) * x1;
        a[2 * i + 1] = bf_hi(wq0[i]) * x0 + bf_hi(wq1[i]) * x1;
      }
      float c4[4], c2[2];
#pragma unroll
      for (int i = 0; i < 4; ++i) c4[i] = tr16(a[i], a[i + 4]);
#pragma unroll
      for (int i = 0; i < 2; ++i) c2[i] = trd<XOR8>(c4[i], c4[i + 2], (lane & 8) != 0);
      float c1 = trd<XOR7>(c2[0], c2[1], (lane & 4) != 0);
      c1 += dpp<XOR2>(c1); c1 += dpp<XOR1>(c1);
      const int col = (lane >> 5) * 8 + ((lane >> 4) & 1) * 4 + ((lane >> 3) & 1) * 2 + ((lane >> 2) & 1);
      if ((lane & 3) == 0) lds[L_QP + u * 16 + col] = c1;
      LGKM0();
      unsigned old = 0u;
      if (lane == 0) old = __hip_atomic_fetch_add(ctl, 1u, RLX_WG);
      old = __builtin_amdgcn_readfirstlane(old);
      asm volatile("" ::: "memory");
      if (old == 6u) {
        float sm = 0.f;
#pragma unroll
        for (int i = 0; i < 7; ++i) sm += lds[L_QP + i * 16 + (lane & 15)];
        const float sn = dpp<XOR1>(sm);
        const unsigned pr = pack_bf(sm, sn);
        const unsigned pr2 = __builtin_bit_cast(unsigned, dpp<XOR2>(__builtin_bit_cast(float, pr)));
        if (lane < 16 && (lane & 3) == 0) {
          const u32x4 gv = {pr, tag, pr2, tag};
          __builtin_amdgcn_raw_buffer_store_b128(gv, wsr, WS_Q1 + (unsigned)((g * 256 + j * 8 + (lane >> 1)) * 8), 0, 16);
        }
        if (lane == 0) __hip_atomic_store(ctl + 2, 1u, RLX_WG);
        STAMP(1);
      }
    }
    {
      float mb2, rd2;
      ln_stats(xf, lv, mb2, rd2);
      const int dd = b < 224 ? 2 * b : 0;
      const float h0 = rbf(rbf(xl[dd] - mb2) * rd2), h1 = rbf(rbf(xl[dd + 1] - mb2) * rd2);
      float y0 = 0.f, y1 = 0.f;
#pragma unroll
      for (int i = 0; i < 4; ++i) {
        const float a1 = (i & 1) ? bf_hi(m1r[i >> 1]) : bf_lo(m1r[i >> 1]);
        const float ab = (i & 1) ? bf_hi(mbr[i >> 1]) : bf_lo(mbr[i >> 1]);
        const float a2 = (i & 1) ? bf_hi(m2r[i >> 1]) : bf_lo(m2r[i >> 1]);
        const float u0 = rbf(rbf(h0 * a1) + ab), u1 = rbf(rbf(h1 * a1) + ab);
        const float z0 = 0.7978845608028654f * (u0 + 0.044715f * u0 * u0 * u0);
        const float z1 = 0.7978845608028654f * (u1 + 0.044715f * u1 * u1 * u1);
        const float g0 = rbf(u0 * __builtin_amdgcn_rcpf(1.f + __expf(-2.f * z0)));
        const float g1 = rbf(u1 * __builtin_amdgcn_rcpf(1.f + __expf(-2.f * z1)));
        y0 += g0 * a2; y1 += g1 * a2;
      }
      y0 = sum64(y0); y1 = sum64(y1);
      if (lane == 0) { lds[L_MLP + 2 * u] = y0; lds[L_MLP + 2 * u + 1] = y1; }
      LGKM0();
      unsigned old = 0u;
      if (lane == 0) old = __hip_atomic_fetch_add(ctl + 6, 1u, RLX_WG);
      old = __builtin_amdgcn_readfirstlane(old);
      asm volatile("" ::: "memory");
      if (old == 6u && b < 224 && lane == 0) {
        float s0 = 0.f, s1 = 0.f;
#pragma unroll
        for (int i = 0; i < 7; ++i) { s0 += lds[L_MLP + 2 * i]; s1 += lds[L_MLP + 2 * i + 1]; }
        const float b2 = bf_lo(p.b2[0]);
        s0 = rbf(rbf(s0) + b2); s1 = rbf(rbf(s1) + b2);
        const u32x4 gv = {__float_as_uint(s0), tag, __float_as_uint(s1), tag};
        __builtin_amdgcn_raw_buffer_store_b128(gv, wsr, WS_MB + (unsigned)(b * 16), 0, 16);
        STAMP(11);
      }
    }
  } else {
    __builtin_amdgcn_sched_barrier(0);
    BAR();
  }
  int pass = (w >= 9) ? 0 : 1;
#pragma clang loop unroll(disable)
  for (; pass < 2; ++pass) {
  pass = __builtin_amdgcn_readfirstlane(pass);
  asm volatile("" : "+s"(pass));
  const bool real = pass != 0;
  if (real && w >= 8) {
    const int u = w - 8;
    if (u == 0) {
      u32x4 r0, r1;
      for (unsigned spins = 0;; ++spins) {
        asm volatile("" ::: "memory");
        r0 = __builtin_amdgcn_raw_buffer_load_b128(wsr, WS_Q1 + (unsigned)((g * 256 + 2 * lane) * 8), 0, 16);
        r1 = __builtin_amdgcn_raw_buffer_load_b128(wsr, WS_Q1 + (unsigned)((g * 256 + 128 + 2 * lane) * 8), 0, 16);
        if (__all((r0.y == tag) & (r0.w == tag) & (r1.y == tag) & (r1.w == tag)) || spins > SPIN_CAP) break;
        __builtin_amdgcn_s_sleep(1);
      }
      STAMP(2);
      *(u32x2*)(lds + L_QS + 2 * lane) = u32x2{r0.x, r0.z};
      *(u32x2*)(lds + L_QS + 128 + 2 * lane) = u32x2{r1.x, r1.z};
      LGKM0();
      if (lane == 0) __hip_atomic_store(ctl + 1, 1u, RLX_WG);
    } else {
      lds_wait_ge(ctl + 1, 1u);
    }
    lds_wait_ge(ctl + 3, (unsigned)(u + 1));
    {
      const int r = lane & 15, gq = lane >> 4;
      const bool qrow = r < 2;
      const float* qp = lds + L_QS + (qrow ? r : 0) * 128 + gq * 4;
      const char* kbase = (const char*)(lds + L_K) + (u * 16 + r) * 512;
      f32x4 acc = {0.f, 0.f, 0.f, 0.f};
#pragma unroll
      for (int s = 0; s < 8; ++s) {
        u32x4 qa = *(const u32x4*)(qp + s * 16);
        if (!qrow) qa = u32x4{0u, 0u, 0u, 0u};
        const int ch = 4 * s + gq;
        const u32x4 kb = *(const u32x4*)(kbase + ((ch & 16) | ((ch ^ r) & 15)) * 16);
        acc = __builtin_amdgcn_mfma_f32_16x16x32_bf16(__builtin_bit_cast(bf16x8, qa), __builtin_bit_cast(bf16x8, kb), acc, 0, 0, 0);
      }
      if (lane < 16) {
        lds[L_S + u * 16 + lane] = rbf(acc[0]) * (0.0625f * LOG2E);
        lds[L_S + 128 + u * 16 + lane] = rbf(acc[1]) * (0.0625f * LOG2E);
      }
    }
    LGKM0();
    unsigned old = 0u;
    if (lane == 0) old = __hip_atomic_fetch_add(ctl + 4, 1u, RLX_WG);
    old = __builtin_amdgcn_readfirstlane(old);
    asm volatile("" ::: "memory");
    if (old == 7u) {
      STAMP(3);
      const f32x4 s4 = *(const f32x4*)(lds + L_S + lane * 4);
      const float mh = max32(fmaxf(fmaxf(s4.x, s4.y), fmaxf(s4.z, s4.w)));
      const float p0 = rbf(__builtin_amdgcn_exp2f(s4.x - mh)), p1 = rbf(__builtin_amdgcn_exp2f(s4.y - mh));
      const float p2 = rbf(__builtin_amdgcn_exp2f(s4.z - mh)), p3 = rbf(__builtin_amdgcn_exp2f(s4.w - mh));
      *(u32x2*)(lds + L_P + lane * 2) = u32x2{pack_bf(p0, p1), pack_bf(p2, p3)};
      LGKM0();
      if (lane == 0) __hip_atomic_store(ctl + 5, 1u, RLX_WG);
      STAMP(12);
      const float e = sum32((p0 + p1) + (p2 + p3));
      if ((lane & 31) == 0) {
        const u32x4 gv = {__float_as_uint(mh), tag, __float_as_uint(e), tag};
        __builtin_amdgcn_raw_buffer_store_b128(gv, wsr, WS_ML + (unsigned)(((g * 32 + j) * 2 + (lane >> 5)) * 16), 0, 16);
      }
    }
  }
  {
    if (real) lds_wait_ge(ctl + 5, 1u);
    const int r = lane & 15, gq = lane >> 4, q = r >> 2, pc = lane & 3;
    const bool prow = r < 2;
    const float* pp = lds + L_P + (prow ? r : 0) * 64 + gq * 4;
    f32x4 acc = {0.f, 0.f, 0.f, 0.f};
#pragma unroll
    for (int s = 0; s < 4; ++s) {
      u32x4 pa = *(const u32x4*)(pp + s * 16);
      if (!prow) pa = u32x4{0u, 0u, 0u, 0u};
      if (real) lds_wait_ge(ctl + 3, (unsigned)(10 + 2 * s));
      const int f = q | ((gq & 1) << 2);
      const int ch = (2 * w + (pc >> 1)) ^ (f << 1);
      const char* vb = (const char*)(lds + L_V) + (32 * s + 8 * gq + q) * 512 + ch * 16 + (pc & 1) * 8;
      const bf16x4 v0 = __builtin_amdgcn_ds_read_tr16_b64_v4bf16((LAS bf16x4*)(vb));
      const bf16x4 v1 = __builtin_amdgcn_ds_read_tr16_b64_v4bf16((LAS bf16x4*)(vb + 4 * 512));
      const bf16x8 vv = {v0[0], v0[1], v0[2], v0[3], v1[0], v1[1], v1[2], v1[3]};
      acc = __builtin_amdgcn_mfma_f32_16x16x32_bf16(__builtin_bit_cast(bf16x8, pa), vv, acc, 0, 0, 0);
    }
    const float an = dpp<XOR1>(acc[0]), bn = dpp<XOR1>(acc[1]);
    if (lane < 16 && real) {
      const bool odd = lane & 1;
      const u32x4 gv = {__float_as_uint(odd ? bn : acc[0]), tag, __float_as_uint(odd ? acc[1] : an), tag};
      __builtin_amdgcn_raw_buffer_store_b128(gv, wsr, WS_O2 + (unsigned)(((g * 32 + j) * 512 + (odd ? 256 : 0) + 16 * w + (lane & 14)) * 8), 0, 16);
    }
    if (w == 9 && real) STAMP(5);
  }
  if ((w >= 4 && w < 8) || !real) {
    const int pi = 8 * (w & 3) + (lane >> 3);
    const unsigned oo = WS_O2 + (unsigned)(((g * 32 + pi) * 512 + j * 16 + (lane & 7) * 2) * 8);
    const unsigned mo = WS_ML + (unsigned)(((g * 32 + (lane & 31)) * 2 + (j >> 4)) * 16);
    u32x4 r0, rm;
    for (unsigned spins = 0;; ++spins) {
      asm volatile("" ::: "memory");
      r0 = __builtin_amdgcn_raw_buffer_load_b128(wsr, oo, 0, 16);
      rm = __builtin_amdgcn_raw_buffer_load_b128(wsr, mo, 0, 16);
      const bool ok = (r0.y == tag) & (r0.w == tag) & (rm.y == tag) & (rm.w == tag);
      if (__all(ok) || spins > SPIN_CAP || !real) break;
      __builtin_amdgcn_s_sleep(1);
    }
    const float mi = __uint_as_float(rm.x), li = __uint_as_float(rm.z);
    const float M = max32(mi);
    const float wi = __builtin_amdgcn_exp2f(mi - M);
    const float L = sum32(wi * li);
    const float ci = wi / L;
    const float c = __shfl(ci, pi);
    float o0 = __uint_as_float(r0.x) * c, o1 = __uint_as_float(r0.z) * c;
    o0 += dpp<XOR8>(o0); o1 += dpp<XOR8>(o1);
    o0 = tr16(o0, o0); o1 = tr16(o1, o1);
    o0 = tr32(o0, o0); o1 = tr32(o1, o1);
    if (lane < 8 && real) *(float2*)(lds + L_R + (w - 4) * 16 + lane * 2) = make_float2(o0, o1);
  }
  if (real) BAR();
  if (w == 0) STAMP(6);
  {
    const float ov = rbf((lds[L_R + w] + lds[L_R + 16 + w]) + (lds[L_R + 32 + w] + lds[L_R + 48 + w]));
    if (lane < 56 && real) {
      float* t = lds + L_T + w * 448 + lane * 8;
      *(f32x4*)(t) = f32x4{bf_lo(wor[0]) * ov, bf_hi(wor[0]) * ov, bf_lo(wor[1]) * ov, bf_hi(wor[1]) * ov};
      *(f32x4*)(t + 4) = f32x4{bf_lo(wor[2]) * ov, bf_hi(wor[2]) * ov, bf_lo(wor[3]) * ov, bf_hi(wor[3]) * ov};
    }
  }
  if (real) BAR();
  if (tid < 224 || !real) {
    const int t2 = real ? tid : lane;
    float s0 = 0.f, s1 = 0.f;
#pragma unroll
    for (int i = 0; i < 16; ++i) { const float2 t = *(const float2*)(lds + L_T + i * 448 + t2 * 2); s0 += t.x; s1 += t.y; }
    const u32x4 gv = {__float_as_uint(s0), tag, __float_as_uint(s1), tag};
    if (real) __builtin_amdgcn_raw_buffer_store_b128(gv, wsr, WS_P3 + (unsigned)((b * 224 + t2) * 16), 0, 16);
    else asm volatile("" :: "v"(gv));
  }
  if (w == 0) STAMP(7);
  if (b < 56) {
    u32x4 r, rb;
    const unsigned xx = *(const unsigned*)(p.x + 8 * b + 2 * (lane & 3));
    for (unsigned spins = 0;; ++spins) {
      asm volatile("" ::: "memory");
      r = __builtin_amdgcn_raw_buffer_load_b128(wsr, WS_P3 + (unsigned)(((tid >> 2) * 224 + 4 * b + (tid & 3)) * 16), 0, 16);
      rb = __builtin_amdgcn_raw_buffer_load_b128(wsr, WS_MB + (unsigned)((4 * b + (tid & 3)) * 16), 0, 16);
      const bool ok = (r.y == tag) & (r.w == tag) & (rb.y == tag) & (rb.w == tag);
      if (__all(ok) || spins > SPIN_CAP || !real) break;
      __builtin_amdgcn_s_sleep(1);
    }
    float s0 = __uint_as_float(r.x), s1 = __uint_as_float(r.z);
    s0 += dpp<XOR7>(dpp<XOR1>(dpp<XOR2>(s0))); s1 += dpp<XOR7>(dpp<XOR1>(dpp<XOR2>(s1)));
    s0 += dpp<XOR8>(s0); s1 += dpp<XOR8>(s1);
    s0 = tr16(s0, s0); s1 = tr16(s1, s1);
    s0 = tr32(s0, s0); s1 = tr32(s1, s1);
    if (lane < 4 && real) *(float2*)(lds + L_F + w * 8 + lane * 2) = make_float2(s0, s1);
    if (real) BAR();
    if (tid < 4 || (!real && lane < 4)) {
      float a0 = 0.f, a1 = 0.f;
#pragma unroll
      for (int i = 0; i < 16; ++i) { const float2 t = *(const float2*)(lds + L_F + i * 8 + lane * 2); a0 += t.x; a1 += t.y; }
      const float r0 = rbf(rbf(bf_lo(xx) + rbf(a0)) + __uint_as_float(rb.x)), r1 = rbf(rbf(bf_hi(xx) + rbf(a1)) + __uint_as_float(rb.z));
      if (real) {
        *(unsigned*)(p.out + 8 * b + 2 * tid) = pack_bf(r0, r1);
        if (b == 0 && tid == 0) __hip_atomic_store((gu32*)(p.ws + WS_EPOCH), tag, RLX_AGENT);
      } else asm volatile("" :: "v"(r0), "v"(r1));
    }
    if (w == 0) STAMP(8);
  }
  }
}
}

extern "C" void kernel_launch(void* const* d_in, const int* in_sizes, int n_in,
                              void* d_out, int out_size, void* d_ws, size_t ws_size,
                              hipStream_t stream) {
  Params p;
  p.x = (const unsigned short*)d_in[0]; p.k = (const unsigned short*)d_in[1]; p.v = (const unsigned short*)d_in[2];
  p.wq = (const unsigned short*)d_in[3]; p.wo = (const unsigned short*)d_in[4];
  p.w1 = (const unsigned short*)d_in[6]; p.b1 = (const unsigned short*)d_in[7];
  p.w2 = (const unsigned short*)d_in[8]; p.b2 = (const unsigned short*)d_in[9];
  p.out = (unsigned short*)d_out; p.ws = (unsigned char*)d_ws;
  hipLaunchKernelGGL(fused_decode, dim3(NWG), dim3(NTHR), 0, stream, p);
}
```

```cpp
#include <hip/hip_runtime.h>
#include <hip/hip_bf16.h>
#include <stdint.h>

typedef unsigned u32x4 __attribute__((ext_vector_type(4)));
typedef unsigned u32x2 __attribute__((ext_vector_type(2)));
typedef float f32x4 __attribute__((ext_vector_type(4)));
typedef __bf16 bf16x2 __attribute__((ext_vector_type(2)));
typedef __bf16 bf16x4 __attribute__((ext_vector_type(4)));
typedef __bf16 bf16x8 __attribute__((ext_vector_type(8)));
#define GAS __attribute__((address_space(1)))
#define LAS __attribute__((address_space(3)))
typedef GAS unsigned long long gu64;
typedef GAS unsigned gu32;
#define RLX_AGENT __ATOMIC_RELAXED, __HIP_MEMORY_SCOPE_AGENT
#define RLX_WG __ATOMIC_RELAXED, __HIP_MEMORY_SCOPE_WORKGROUP

namespace {
constexpr int DM = 448, SK = 32768, DH = 256, WIN = 4096;
constexpr int NWG = 256, NTHR = 1024;
constexpr unsigned WS_EPOCH = 0;
constexpr unsigned WS_Q1 = 4096;
constexpr unsigned WS_ML = WS_Q1 + 8 * 256 * 8;
constexpr unsigned WS_O2 = WS_ML + 8 * 32 * 4 * 8;
constexpr unsigned WS_P3 = WS_O2 + 8 * 32 * 512 * 8;
constexpr unsigned WS_MB = WS_P3 + 256 * 448 * 8;
constexpr unsigned WS_END = WS_MB + 448 * 8;
constexpr unsigned SPIN_CAP = 1u << 20;
#define LW 32
#define KPRE 4
constexpr float LOG2E = 1.4426950408889634f;

constexpr int L_K = 0;
constexpr int L_V = 16384;
constexpr int L_CTL = 32768;
constexpr int L_X = L_CTL + 16;
constexpr int L_QP = L_X + 7 * 448;
constexpr int L_QS = L_QP + 7 * 16;
constexpr int L_S = L_QS + 256;
constexpr int L_P = L_S + 256;
constexpr int L_MLP = L_P + 128;
constexpr int L_R = L_MLP + 16;
constexpr int L_F = L_R + 64;
constexpr int L_END = L_F + 128;
constexpr int L_T = L_V;

__device__ __forceinline__ float bf_lo(unsigned w) { return __builtin_bit_cast(float, w << 16); }
__device__ __forceinline__ float bf_hi(unsigned w) { return __builtin_bit_cast(float, w & 0xffff0000u); }
__device__ __forceinline__ float rbf(float f) { return (float)(__bf16)f; }
__device__ __forceinline__ unsigned pack_bf(float lo, float hi) {
  bf16x2 v; v.x = (__bf16)lo; v.y = (__bf16)hi; return __builtin_bit_cast(unsigned, v);
}
template <int CTRL> __device__ __forceinline__ float dpp(float x) {
  return __builtin_bit_cast(float, __builtin_amdgcn_mov_dpp(__builtin_bit_cast(int, x), CTRL, 0xf, 0xf, true));
}
constexpr int XOR1 = 0xB1, XOR2 = 0x4E, XOR7 = 0x141, XOR8 = 0x128;
__device__ __forceinline__ float tr16(float lo, float hi) {
  auto s = __builtin_amdgcn_permlane16_swap(__float_as_uint(lo), __float_as_uint(hi), false, false);
  return __uint_as_float(s[0]) + __uint_as_float(s[1]);
}
__device__ __forceinline__ float tr32(float lo, float hi) {
  auto s = __builtin_amdgcn_permlane32_swap(__float_as_uint(lo), __float_as_uint(hi), false, false);
  return __uint_as_float(s[0]) + __uint_as_float(s[1]);
}
template <int CTRL> __device__ __forceinline__ float trd(float lo, float hi, bool bit) {
  const float keep = bit ? hi : lo, send = bit ? lo : hi;
  return keep + dpp<CTRL>(send);
}
__device__ __forceinline__ float sum16(float v) {
  v += dpp<XOR1>(v); v += dpp<XOR2>(v); v += dpp<XOR7>(v); v += dpp<XOR8>(v);
  return v;
}
__device__ __forceinline__ float sum32(float v) { v = sum16(v); return tr16(v, v); }
__device__ __forceinline__ float sum64(float v) { v = sum32(v); return tr32(v, v); }
__device__ __forceinline__ float max32(float v) {
  v = fmaxf(v, dpp<XOR1>(v)); v = fmaxf(v, dpp<XOR2>(v)); v = fmaxf(v, dpp<XOR7>(v)); v = fmaxf(v, dpp<XOR8>(v));
  auto s = __builtin_amdgcn_permlane16_swap(__float_as_uint(v), __float_as_uint(v), false, false);
  return fmaxf(__uint_as_float(s[0]), __uint_as_float(s[1]));
}
__device__ __forceinline__ u32x4 ntload(const void* p) { return __builtin_nontemporal_load((const u32x4*)p); }
#define LGKM0() asm volatile("s_waitcnt lgkmcnt(0)" ::: "memory")
#define BAR() do { asm volatile("s_waitcnt lgkmcnt(0)" ::: "memory"); __builtin_amdgcn_s_barrier(); asm volatile("" ::: "memory"); } while (0)
__device__ __forceinline__ void lds_wait_ge(LAS unsigned* w, unsigned v) {
  for (unsigned spins = 0; __hip_atomic_load(w, RLX_WG) < v && spins < (SPIN_CAP << 4); ++spins) __builtin_amdgcn_s_sleep(1);
  asm volatile("" ::: "memory");
}
__device__ __forceinline__ void lds_put(LAS unsigned* w, unsigned v) {
  asm volatile("ds_write_b32 %0, %1" :: "v"((unsigned)(uintptr_t)w), "v"(v) : "memory");
}
__device__ __forceinline__ unsigned lds_get(LAS unsigned* w) {
  unsigned r; asm volatile("ds_read_b32 %0, %1\n\ts_waitcnt lgkmcnt(0)" : "=v"(r) : "v"((unsigned)(uintptr_t)w) : "memory"); return r;
}

struct Params {
  const unsigned short* x; const unsigned short* k; const unsigned short* v; const unsigned short* wq; const unsigned short* wo;
  const unsigned short* w1; const unsigned short* b1; const unsigned short* w2; const unsigned short* b2;
  unsigned short* out; unsigned char* ws;
};

__device__ __forceinline__ void ln_stats(const float (&xf)[8], float lv, float& mb, float& rd) {
  float s = 0.f;
#pragma unroll
  for (int i = 0; i < 8; ++i) s += xf[i];
  const float mean = sum64(s * lv) * (1.f / DM);
  float q2 = 0.f;
#pragma unroll
  for (int i = 0; i < 8; ++i) { const float d = xf[i] - mean; q2 += d * d; }
  const float var = sum64(q2 * lv) * (1.f / DM);
  mb = rbf(mean); rd = 1.f / rbf(sqrtf(rbf(rbf(var) + 1e-5f)));
}

__global__ void __launch_bounds__(NTHR, 4) fused_decode(Params p) {
  __shared__ __attribute__((aligned(16))) float lds[L_END];
  const int b = blockIdx.x, g = b & 7, j = b >> 3;
  const int tid = threadIdx.x, lane = tid & 63, w = __builtin_amdgcn_readfirstlane(tid >> 6);
  const __amdgpu_buffer_rsrc_t wsr = __builtin_amdgcn_make_buffer_rsrc(p.ws, 0, WS_END, 0x00020000);
  gu64* const wsg = (gu64*)p.ws;
  LAS unsigned* const ctl = (LAS unsigned*)(lds + L_CTL);
  if (tid < 8) ctl[tid] = 0u;
  unsigned eoff = WS_EPOCH; asm volatile("" : "+v"(eoff));
  unsigned tagraw = 0u;
  if (w != 0) tagraw = __builtin_amdgcn_raw_buffer_load_b32(wsr, eoff, 0, 16);
#define tag (tagraw + 1u)
  u32x4 wor = {0u, 0u, 0u, 0u};
  const unsigned short* const wop = p.wo + (size_t)(g * 512 + j * 16 + w) * DM + (lane < 56 ? lane : 55) * 8;
  if (w != 0) wor = ntload(wop);

  if (w == 0) {
    const size_t kvo = ((size_t)g * SK + (SK - WIN) + j * 128) * DH;
    int ko[8], vo[8];
#pragma unroll
    for (int i = 0; i < 8; ++i) {
      const int row = 2 * i + (lane >> 5), f = (row & 3) | (((row >> 3) & 1) << 2);
      ko[i] = row * 256 + (((lane & 31) ^ row) * 8);
      vo[i] = row * 256 + (((lane & 31) ^ (f << 1)) * 8);
    }
    BAR();
#pragma unroll 1
    for (int c = 0; c < 16; ++c) {
      const unsigned short* src = (c < 8 ? p.k : p.v) + kvo + (c & 7) * 4096;
      if (c == KPRE)
        for (unsigned spins = 0; __builtin_amdgcn_readfirstlane(lds_get(ctl + 2)) == 0u && spins < (SPIN_CAP << 4); ++spins) __builtin_amdgcn_s_sleep(1);
#pragma unroll
      for (int i = 0; i < 8; ++i)
        __builtin_amdgcn_global_load_lds((const unsigned*)(src + (c < 8 ? ko[i] : vo[i])), (LAS unsigned*)(lds + (c * 8 + i) * 256), 16, 0, 2);
      if (8 * (c + 1) >= LW) {
        asm volatile("s_waitcnt vmcnt(%0)" :: "n"(LW - 8) : "memory");
        lds_put(ctl + 3, (unsigned)(c + 2 - LW / 8));
      }
    }
    tagraw = __builtin_amdgcn_raw_buffer_load_b32(wsr, eoff, 0, 16);
    wor = ntload(wop);
#pragma unroll
    for (int t = LW / 8 - 2; t >= 0; --t) {
      asm volatile("s_waitcnt vmcnt(%0)" :: "n"(8 * t + 2) : "memory");
      lds_put(ctl + 3, (unsigned)(16 - t));
    }
  } else if (w < 8) {
    const int u = w - 1;
    const u32x4 xr = *(const u32x4*)(p.x + (lane < 56 ? lane : 55) * 8);
    const unsigned short* s = p.wq + (size_t)(u * 64 + (lane & 31)) * 4096 + g * 512 + j * 16 + (lane >> 5) * 8;
    const u32x4 wq0 = ntload(s), wq1 = ntload(s + 32 * 4096);
    const int fo = (u * 64 + lane) * 4;
    const u32x2 m1r = *(const u32x2*)(p.w1 + fo), mbr = *(const u32x2*)(p.b1 + fo), m2r = *(const u32x2*)(p.w2 + fo);
    __builtin_amdgcn_sched_barrier(0);
    BAR();
    const float lv = lane < 56 ? 1.f : 0.f;
    float* const xl = lds + L_X + u * 448;
    float xf[8], mb, rd;
#pragma unroll
    for (int i = 0; i < 4; ++i) { xf[2 * i] = bf_lo(xr[i]); xf[2 * i + 1] = bf_hi(xr[i]); }
    ln_stats(xf, lv, mb, rd);
#pragma unroll
    for (int i = 0; i < 8; ++i) xf[i] = rbf(rbf(xf[i] - mb) * rd);
    if (lane < 56) {
      *(f32x4*)(xl + lane * 8) = f32x4{xf[0], xf[1], xf[2], xf[3]};
      *(f32x4*)(xl + lane * 8 + 4) = f32x4{xf[4], xf[5], xf[6], xf[7]};
    }
    LGKM0();
    {
      const float x0 = xl[u * 64 + (lane & 31)], x1 = xl[u * 64 + 32 + (lane & 31)];
      float a[8];
#pragma unroll
      for (int i = 0; i < 4; ++i) {
        a[2 * i] = bf_lo(wq0[i]) * x0 + bf_lo(wq1[i]) * x1;
        a[2 * i + 1] = bf_hi(wq0[i]) * x0 + bf_hi(wq1[i]) * x1;
      }
      float c4[4], c2[2];
#pragma unroll
      for (int i = 0; i < 4; ++i) c4[i] = tr16(a[i], a[i + 4]);
#pragma unroll
      for (int i = 0; i < 2; ++i) c2[i] = trd<XOR8>(c4[i], c4[i + 2], (lane & 8) != 0);
      float c1 = trd<XOR7>(c2[0], c2[1], (lane & 4) != 0);
      c1 += dpp<XOR2>(c1); c1 += dpp<XOR1>(c1);
      const int col = (lane >> 5) * 8 + ((lane >> 4) & 1) * 4 + ((lane >> 3) & 1) * 2 + ((lane >> 2) & 1);
      if ((lane & 3) == 0) lds[L_QP + u * 16 + col] = c1;
      LGKM0();
      unsigned old = 0u;
      if (lane == 0) old = __hip_atomic_fetch_add(ctl, 1u, RLX_WG);
      old = __builtin_amdgcn_readfirstlane(old);
      asm volatile("" ::: "memory");
      if (old == 6u) {
        float sm = 0.f;
#pragma unroll
        for (int i = 0; i < 7; ++i) sm += lds[L_QP + i * 16 + (lane & 15)];
        const float sn = dpp<XOR1>(sm);
        const unsigned pr = pack_bf(sm, sn);
        const unsigned pr2 = __builtin_bit_cast(unsigned, dpp<XOR2>(__builtin_bit_cast(float, pr)));
        if (lane < 16 && (lane & 3) == 0) {
          const u32x4 gv = {pr, tag, pr2, tag};
          __builtin_amdgcn_raw_buffer_store_b128(gv, wsr, WS_Q1 + (unsigned)((g * 256 + j * 8 + (lane >> 1)) * 8), 0, 16);
        }
        if (lane == 0) __hip_atomic_store(ctl + 2, 1u, RLX_WG);
      }
    }
    {
      float mb2, rd2;
      ln_stats(xf, lv, mb2, rd2);
      const int dd = b < 224 ? 2 * b : 0;
      const float h0 = rbf(rbf(xl[dd] - mb2) * rd2), h1 = rbf(rbf(xl[dd + 1] - mb2) * rd2);
      float y0 = 0.f, y1 = 0.f;
#pragma unroll
      for (int i = 0; i < 4; ++i) {
        const float a1 = (i & 1) ? bf_hi(m1r[i >> 1]) : bf_lo(m1r[i >> 1]);
        const float ab = (i & 1) ? bf_hi(mbr[i >> 1]) : bf_lo(mbr[i >> 1]);
        const float a2 = (i & 1) ? bf_hi(m2r[i >> 1]) : bf_lo(m2r[i >> 1]);
        const float u0 = rbf(rbf(h0 * a1) + ab), u1 = rbf(rbf(h1 * a1) + ab);
        const float z0 = 0.7978845608028654f * (u0 + 0.044715f * u0 * u0 * u0);
        const float z1 = 0.7978845608028654f * (u1 + 0.044715f * u1 * u1 * u1);
        const float g0 = rbf(u0 * __builtin_amdgcn_rcpf(1.f + __expf(-2.f * z0)));
        const float g1 = rbf(u1 * __builtin_amdgcn_rcpf(1.f + __expf(-2.f * z1)));
        y0 += g0 * a2; y1 += g1 * a2;
      }
      y0 = sum64(y0); y1 = sum64(y1);
      if (lane == 0) { lds[L_MLP + 2 * u] = y0; lds[L_MLP + 2 * u + 1] = y1; }
      LGKM0();
      unsigned old = 0u;
      if (lane == 0) old = __hip_atomic_fetch_add(ctl + 6, 1u, RLX_WG);
      old = __builtin_amdgcn_readfirstlane(old);
      asm volatile("" ::: "memory");
      if (old == 6u && b < 224 && lane == 0) {
        float s0 = 0.f, s1 = 0.f;
#pragma unroll
        for (int i = 0; i < 7; ++i) { s0 += lds[L_MLP + 2 * i]; s1 += lds[L_MLP + 2 * i + 1]; }
        const float b2 = bf_lo(p.b2[0]);
        s0 = rbf(rbf(s0) + b2); s1 = rbf(rbf(s1) + b2);
        const u32x4 gv = {__float_as_uint(s0), tag, __float_as_uint(s1), tag};
        __builtin_amdgcn_raw_buffer_store_b128(gv, wsr, WS_MB + (unsigned)(b * 16), 0, 16);
      }
    }
  } else {
    const int u = w - 8;
    __builtin_amdgcn_sched_barrier(0);
    BAR();
    if (u == 0) {
      u32x4 r0, r1;
      for (unsigned spins = 0;; ++spins) {
        asm volatile("" ::: "memory");
        r0 = __builtin_amdgcn_raw_buffer_load_b128(wsr, WS_Q1 + (unsigned)((g * 256 + 2 * lane) * 8), 0, 16);
        r1 = __builtin_amdgcn_raw_buffer_load_b128(wsr, WS_Q1 + (unsigned)((g * 256 + 128 + 2 * lane) * 8), 0, 16);
        if (__all((r0.y == tag) & (r0.w == tag) & (r1.y == tag) & (r1.w == tag)) || spins > SPIN_CAP) break;
        __builtin_amdgcn_s_sleep(1);
      }
      *(u32x2*)(lds + L_QS + 2 * lane) = u32x2{r0.x, r0.z};
      *(u32x2*)(lds + L_QS + 128 + 2 * lane) = u32x2{r1.x, r1.z};
      LGKM0();
      if (lane == 0) __hip_atomic_store(ctl + 1, 1u, RLX_WG);
    } else {
      lds_wait_ge(ctl + 1, 1u);
    }
    lds_wait_ge(ctl + 3, (unsigned)(u + 1));
    {
      const int r = lane & 15, gq = lane >> 4;
      const bool qrow = r < 2;
      const float* qp = lds + L_QS + (qrow ? r : 0) * 128 + gq * 4;
      const char* kbase = (const char*)(lds + L_K) + (u * 16 + r) * 512;
      f32x4 acc = {0.f, 0.f, 0.f, 0.f};
#pragma unroll
      for (int s = 0; s < 8; ++s) {
        u32x4 qa = *(const u32x4*)(qp + s * 16);
        if (!qrow) qa = u32x4{0u, 0u, 0u, 0u};
        const int ch = 4 * s + gq;
        const u32x4 kb = *(const u32x4*)(kbase + ((ch & 16) | ((ch ^ r) & 15)) * 16);
        acc = __builtin_amdgcn_mfma_f32_16x16x32_bf16(__builtin_bit_cast(bf16x8, qa), __builtin_bit_cast(bf16x8, kb), acc, 0, 0, 0);
      }
      if (lane < 16) {
        lds[L_S + u * 16 + lane] = rbf(acc[0]) * (0.0625f * LOG2E);
        lds[L_S + 128 + u * 16 + lane] = rbf(acc[1]) * (0.0625f * LOG2E);
      }
    }
    LGKM0();
    unsigned old = 0u;
    if (lane == 0) old = __hip_atomic_fetch_add(ctl + 4, 1u, RLX_WG);
    old = __builtin_amdgcn_readfirstlane(old);
    asm volatile("" ::: "memory");
    if (old == 7u) {
      const f32x4 s4 = *(const f32x4*)(lds + L_S + lane * 4);
      const float mh = max32(fmaxf(fmaxf(s4.x, s4.y), fmaxf(s4.z, s4.w)));
      const float p0 = rbf(__builtin_amdgcn_exp2f(s4.x - mh)), p1 = rbf(__builtin_amdgcn_exp2f(s4.y - mh));
      const float p2 = rbf(__builtin_amdgcn_exp2f(s4.z - mh)), p3 = rbf(__builtin_amdgcn_exp2f(s4.w - mh));
      *(u32x2*)(lds + L_P + lane * 2) = u32x2{pack_bf(p0, p1), pack_bf(p2, p3)};
      LGKM0();
      if (lane == 0) __hip_atomic_store(ctl + 5, 1u, RLX_WG);
      const float e = sum32((p0 + p1) + (p2 + p3));
      if ((lane & 31) == 0) {
        const u32x4 gv = {__float_as_uint(mh), tag, __float_as_uint(e), tag};
        __builtin_amdgcn_raw_buffer_store_b128(gv, wsr, WS_ML + (unsigned)(((g * 32 + j) * 2 + (lane >> 5)) * 16), 0, 16);
      }
    }
  }
  {
    lds_wait_ge(ctl + 5, 1u);
    const int r = lane & 15, gq = lane >> 4, q = r >> 2, pc = lane & 3;
    const bool prow = r < 2;
    const float* pp = lds + L_P + (prow ? r : 0) * 64 + gq * 4;
    f32x4 acc = {0.f, 0.f, 0.f, 0.f};
#pragma unroll
    for (int s = 0; s < 4; ++s) {
      u32x4 pa = *(const u32x4*)(pp + s * 16);
      if (!prow) pa = u32x4{0u, 0u, 0u, 0u};
      lds_wait_ge(ctl + 3, (unsigned)(10 + 2 * s));
      const int f = q | ((gq & 1) << 2);
      const int ch = (2 * w + (pc >> 1)) ^ (f << 1);
      const char* vb = (const char*)(lds + L_V) + (32 * s + 8 * gq + q) * 512 + ch * 16 + (pc & 1) * 8;
      const bf16x4 v0 = __builtin_amdgcn_ds_read_tr16_b64_v4bf16((LAS bf16x4*)(vb));
      const bf16x4 v1 = __builtin_amdgcn_ds_read_tr16_b64_v4bf16((LAS bf16x4*)(vb + 4 * 512));
      const bf16x8 vv = {v0[0], v0[1], v0[2], v0[3], v1[0], v1[1], v1[2], v1[3]};
      acc = __builtin_amdgcn_mfma_f32_16x16x32_bf16(__builtin_bit_cast(bf16x8, pa), vv, acc, 0, 0, 0);
    }
    const float an = dpp<XOR1>(acc[0]), bn = dpp<XOR1>(acc[1]);
    if (lane < 16) {
      const bool odd = lane & 1;
      const u32x4 gv = {__float_as_uint(odd ? bn : acc[0]), tag, __float_as_uint(odd ? acc[1] : an), tag};
      __builtin_amdgcn_raw_buffer_store_b128(gv, wsr, WS_O2 + (unsigned)(((g * 32 + j) * 512 + (odd ? 256 : 0) + 16 * w + (lane & 14)) * 8), 0, 16);
    }
  }
  if (w >= 4 && w < 8) {
    const int pi = 8 * (w - 4) + (lane >> 3);
    const unsigned oo = WS_O2 + (unsigned)(((g * 32 + pi) * 512 + j * 16 + (lane & 7) * 2) * 8);
    const unsigned mo = WS_ML + (unsigned)(((g * 32 + (lane & 31)) * 2 + (j >> 4)) * 16);
    u32x4 r0, rm;
    for (unsigned spins = 0;; ++spins) {
      asm volatile("" ::: "memory");
      r0 = __builtin_amdgcn_raw_buffer_load_b128(wsr, oo, 0, 16);
      rm = __builtin_amdgcn_raw_buffer_load_b128(wsr, mo, 0, 16);
      const bool ok = (r0.y == tag) & (r0.w == tag) & (rm.y == tag) & (rm.w == tag);
      if (__all(ok) || spins > SPIN_CAP) break;
      __builtin_amdgcn_s_sleep(1);
    }
    const float mi = __uint_as_float(rm.x), li = __uint_as_float(rm.z);
    const float M = max32(mi);
    const float wi = __builtin_amdgcn_exp2f(mi - M);
    const float L = sum32(wi * li);
    const float ci = wi / L;
    const float c = __shfl(ci, pi);
    float o0 = __uint_as_float(r0.x) * c, o1 = __uint_as_float(r0.z) * c;
    o0 += dpp<XOR8>(o0); o1 += dpp<XOR8>(o1);
    o0 = tr16(o0, o0); o1 = tr16(o1, o1);
    o0 = tr32(o0, o0); o1 = tr32(o1, o1);
    if (lane < 8) *(float2*)(lds + L_R + (w - 4) * 16 + lane * 2) = make_float2(o0, o1);
  }
  BAR();
  {
    const float ov = rbf((lds[L_R + w] + lds[L_R + 16 + w]) + (lds[L_R + 32 + w] + lds[L_R + 48 + w]));
    if (lane < 56) {
      float* t = lds + L_T + w * 448 + lane * 8;
      *(f32x4*)(t) = f32x4{bf_lo(wor[0]) * ov, bf_hi(wor[0]) * ov, bf_lo(wor[1]) * ov, bf_hi(wor[1]) * ov};
      *(f32x4*)(t + 4) = f32x4{bf_lo(wor[2]) * ov, bf_hi(wor[2]) * ov, bf_lo(wor[3]) * ov, bf_hi(wor[3]) * ov};
    }
  }
  BAR();
  if (tid < 224) {
    float s0 = 0.f, s1 = 0.f;
#pragma unroll
    for (int i = 0; i < 16; ++i) { const float2 t = *(const float2*)(lds + L_T + i * 448 + tid * 2); s0 += t.x; s1 += t.y; }
    const u32x4 gv = {__float_as_uint(s0), tag, __float_as_uint(s1), tag};
    __builtin_amdgcn_raw_buffer_store_b128(gv, wsr, WS_P3 + (unsigned)((b * 224 + tid) * 16), 0, 16);
  }
  if (b < 56) {
    u32x4 r, rb;
    const unsigned xx = *(const unsigned*)(p.x + 8 * b + 2 * (lane & 3));
    for (unsigned spins = 0;; ++spins) {
      asm volatile("" ::: "memory");
      r = __builtin_amdgcn_raw_buffer_load_b128(wsr, WS_P3 + (unsigned)(((tid >> 2) * 224 + 4 * b + (tid & 3)) * 16), 0, 16);
      rb = __builtin_amdgcn_raw_buffer_load_b128(wsr, WS_MB + (unsigned)((4 * b + (tid & 3)) * 16), 0, 16);
      const bool ok = (r.y == tag) & (r.w == tag) & (rb.y == tag) & (rb.w == tag);
      if (__all(ok) || spins > SPIN_CAP) break;
      __builtin_amdgcn_s_sleep(1);
    }
    float s0 = __uint_as_float(r.x), s1 = __uint_as_float(r.z);
    s0 += dpp<XOR7>(dpp<XOR1>(dpp<XOR2>(s0))); s1 += dpp<XOR7>(dpp<XOR1>(dpp<XOR2>(s1)));
    s0 += dpp<XOR8>(s0); s1 += dpp<XOR8>(s1);
    s0 = tr16(s0, s0); s1 = tr16(s1, s1);
    s0 = tr32(s0, s0); s1 = tr32(s1, s1);
    if (lane < 4) *(float2*)(lds + L_F + w * 8 + lane * 2) = make_float2(s0, s1);
    BAR();
    if (tid < 4) {
      float a0 = 0.f, a1 = 0.f;
#pragma unroll
      for (int i = 0; i < 16; ++i) { const float2 t = *(const float2*)(lds + L_F + i * 8 + tid * 2); a0 += t.x; a1 += t.y; }
      const float r0 = rbf(rbf(bf_lo(xx) + rbf(a0)) + __uint_as_float(rb.x)), r1 = rbf(rbf(bf_hi(xx) + rbf(a1)) + __uint_as_float(rb.z));
      *(unsigned*)(p.out + 8 * b + 2 * tid) = pack_bf(r0, r1);
      if (b == 0 && tid == 0) __hip_atomic_store((gu32*)(p.ws + WS_EPOCH), tag, RLX_AGENT);
    }
  }
}
}

extern "C" void kernel_launch(void* const* d_in, const int* in_sizes, int n_in,
                              void* d_out, int out_size, void* d_ws, size_t ws_size,
                              hipStream_t stream) {
  Params p;
  p.x = (const unsigned short*)d_in[0]; p.k = (const unsigned short*)d_in[1]; p.v = (const unsigned short*)d_in[2];
  p.wq = (const unsigned short*)d_in[3]; p.wo = (const unsigned short*)d_in[4];
  p.w1 = (const unsigned short*)d_in[6]; p.b1 = (const unsigned short*)d_in[7];
  p.w2 = (const unsigned short*)d_in[8]; p.b2 = (const unsigned short*)d_in[9];
  p.out = (unsigned short*)d_out; p.ws = (unsigned char*)d_ws;
  hipLaunchKernelGGL(fused_decode, dim3(NWG), dim3(NTHR), 0, stream, p);
}
```
